```python
import torch

def _softmax(x, axis=-1):
    return torch.softmax(x, dim=axis)
def _gelu(x):
    return torch.nn.functional.gelu(x, approximate="tanh")
def _silu(x):
    return torch.nn.functional.silu(x)
def _sigmoid(x):
    return torch.sigmoid(x)
def _layernorm(x, axis=-1, eps=1e-5):
    m = x.mean(dim=axis, keepdim=True)
    v = x.var(dim=axis, keepdim=True, unbiased=False)
    return (x - m) / torch.sqrt(v + eps)
def _scatter_add(tgt, idx, src, axis=0):
    return tgt.clone().index_add_(axis, idx, src)
def _segment_sum(src, seg, n):
    out = torch.zeros((n,) + src.shape[1:], dtype=src.dtype)
    return out.index_add_(0, seg, src)
def _pad(x, pw):
    flat = tuple(v for pair in reversed(pw) for v in pair)
    return torch.nn.functional.pad(x, flat)

kernel_name = 'interleave_layers__sliding_local_dilated__glu_gated_swiglu__prompt'

def setup_inputs():

    _w = (lambda a: (lambda t: t.to(torch.bfloat16) if t.is_floating_point() else t)(torch.as_tensor(a)))
    def _A_setup():
        B, S, D = 1, 2048, 448
        return {
            "x": _w((lambda *s: torch.randn(*s, dtype=torch.bfloat16) * 0.05)(B, S, D)),
            "wqkv": _w((lambda *s: torch.randn(*s, dtype=torch.bfloat16) * 0.05)(D, 3 * D)),
            "wo": _w((lambda *s: torch.randn(*s, dtype=torch.bfloat16) * 0.05)(D, D)),
        }
    _A_in = _A_setup()

    def _B_setup():
        B, S, D, F = 1, 2048, 448, 1792
        return {
            "x": _w((lambda *s: torch.randn(*s, dtype=torch.bfloat16) * 0.05)(B, S, D)),
            "w1": _w((lambda *s: torch.randn(*s, dtype=torch.bfloat16) * 0.8)(D, F) * (2.0 / D) ** 0.5),
            "w3": _w((lambda *s: torch.randn(*s, dtype=torch.bfloat16) * 0.8)(D, F) * (2.0 / D) ** 0.5),
            "w2": _w((lambda *s: torch.randn(*s, dtype=torch.bfloat16) * 0.8)(F, D) * (2.0 / F) ** 0.5),
        }
    _B_in = _B_setup()

    return {
        "A_x": _A_in["x"],
        "A_wqkv": _A_in["wqkv"],
        "A_wo": _A_in["wo"],
        "B_x": _B_in["x"],
        "B_w1": _B_in["w1"],
        "B_w3": _B_in["w3"],
        "B_w2": _B_in["w2"],
    }

def reference(A_x, A_wqkv, A_wo, B_x, B_w1, B_w3, B_w2):
    def _A_ref(x, wqkv, wo):
        B, S, D = x.shape
        H = 16; Dh = D // H
        W, dil = 8, 2
        qkv = (lambda x, *d: x.permute(*d))((x @ wqkv).reshape(B, S, 3, H, Dh), 2, 0, 3, 1, 4)
        q, k, v = qkv[0], qkv[1], qkv[2]
        scale = Dh ** -0.5
        pos = torch.arange(S)
        off = torch.arange(W) * dil
        idx = pos[:, None] - off[None, :]
        mask = idx >= 0
        idx_c = torch.where(mask, idx, 0).reshape(-1)
        kg = k[:, :, idx_c, :].reshape(B, H, S, W, Dh)
        vg = v[:, :, idx_c, :].reshape(B, H, S, W, Dh)
        logits = torch.einsum("bhsd,bhswd->bhsw", q, kg) * scale
        logits = torch.where(mask[None, None, :, :], logits, -1e9)
        attn = _softmax(logits, axis=-1)
        o = torch.einsum("bhsw,bhswd->bhsd", attn, vg)
        return (lambda x, *d: x.permute(*d))(o, 0, 2, 1, 3).reshape(B, S, D) @ wo

    def _B_ref(x, w1, w3, w2):
        gate = torch.einsum("bsd,df->bsf", x, w1)
        up = torch.einsum("bsd,df->bsf", x, w3)
        h = _silu(gate) * up
        out = torch.einsum("bsf,fd->bsd", h, w2)
        return out

    x = A_x
    x = x + _A_ref(_layernorm(x, axis=-1), A_wqkv, A_wo)
    x = x + _B_ref(_layernorm(x, axis=-1), B_w1, B_w3, B_w2)
    x = x + _A_ref(_layernorm(x, axis=-1), A_wqkv, A_wo)
    x = x + _B_ref(_layernorm(x, axis=-1), B_w1, B_w3, B_w2)
    return x
```

```cpp
#include <hip/hip_runtime.h>
#include <hip/hip_bf16.h>
#include <stdint.h>

typedef __attribute__((ext_vector_type(8))) __bf16 bf16x8;
typedef __attribute__((ext_vector_type(2))) __bf16 bf16x2;
typedef __attribute__((ext_vector_type(4))) float f32x4;
typedef __attribute__((ext_vector_type(2))) float f32x2;
typedef __attribute__((ext_vector_type(4))) unsigned u32x4;
typedef __attribute__((ext_vector_type(2))) unsigned u32x2;
typedef __attribute__((address_space(1))) unsigned gu32;
typedef __amdgpu_buffer_rsrc_t rsrc_t;

#define XMAP 1
#define N13E 28
#define PF3 3
#define N2B 14
#define NH0 8

constexpr unsigned O_CNT = 0;
constexpr unsigned O_CSQ = 32768;
constexpr unsigned O_CS13 = O_CSQ + 6144;
constexpr unsigned O_ST = 65536;
constexpr unsigned ST_SZ = 65536;
constexpr unsigned O_WQ = O_ST + 4 * ST_SZ;
constexpr unsigned O_WO = O_WQ + 16 * 14 * 6 * 1024;
constexpr unsigned O_W13 = O_WO + 4 * 16 * 7 * 1024;
constexpr unsigned O_W2 = O_W13 + 16 * 14 * 14 * 1024;
constexpr unsigned O_X = O_W2 + 4 * 56 * 7 * 1024;
constexpr unsigned X_SZ = 128 * 14 * 1024;
constexpr unsigned O_O = O_X + 4 * X_SZ;
constexpr unsigned O_SZ = 128 * 16 * 1024;
constexpr unsigned O_H = O_O + 2 * O_SZ;
constexpr unsigned H_SZ = 128 * 56 * 1024;
constexpr unsigned WS_END = O_H + 2 * H_SZ;
constexpr unsigned L_RED = 86016, L_ST = 143360, L_EX = 144640, L_END = L_EX + 10240;

#define DEV __device__ __forceinline__
template <int N> struct IC { static constexpr int v = N; };
template <int B, int E, class F> DEV void sfor(F&& f) { if constexpr (B < E) { f(IC<B>{}); sfor<B + 1, E>(f); } }

DEV f32x4 mfma(u32x4 a, u32x4 b, f32x4 c) {
  return __builtin_amdgcn_mfma_f32_16x16x32_bf16(__builtin_bit_cast(bf16x8, a), __builtin_bit_cast(bf16x8, b), c, 0, 0, 0);
}
DEV unsigned pk(float lo, float hi) { f32x2 v = {lo, hi}; return __builtin_bit_cast(unsigned, __builtin_convertvector(v, bf16x2)); }
DEV float blo(unsigned u) { return __uint_as_float(u << 16); }
DEV float bhi(unsigned u) { return __uint_as_float(u & 0xffff0000u); }
template <int AUX> DEV u32x4 ld16(rsrc_t r, unsigned voff, unsigned soff) { return __builtin_amdgcn_raw_buffer_load_b128(r, voff, soff, AUX); }
template <int AUX> DEV void st16(u32x4 v, rsrc_t r, unsigned voff, unsigned soff) { __builtin_amdgcn_raw_buffer_store_b128(v, r, voff, soff, AUX); }
DEV unsigned opq(unsigned x) { x = __builtin_amdgcn_readfirstlane(x); asm volatile("" : "+s"(x)); return x; }
template <int AUX, int F> DEV u32x4 ldf(rsrc_t r, unsigned v16, unsigned base) { return ld16<AUX>(r, v16 + (F & 3) * 1024, base + (F >> 2) * 4096); }
template <bool SC1> DEV void dma16(rsrc_t r, unsigned voff, unsigned soff, unsigned lds) {
  if constexpr (SC1) asm volatile("s_mov_b32 m0, %0\n\ts_nop 0\n\tbuffer_load_dwordx4 %1, %2, %3 offen sc1 lds" :: "s"(lds), "v"(voff), "s"(r), "s"(soff) : "memory");
  else asm volatile("s_mov_b32 m0, %0\n\ts_nop 0\n\tbuffer_load_dwordx4 %1, %2, %3 offen lds" :: "s"(lds), "v"(voff), "s"(r), "s"(soff) : "memory");
}
template <int N> DEV void wait_vm() { asm volatile("s_waitcnt vmcnt(%0)" :: "i"(N > 63 ? 63 : N) : "memory"); }
DEV void bar() { asm volatile("s_waitcnt lgkmcnt(0)\n\ts_barrier" ::: "memory"); }
DEV float x32(float x, float& y) { u32x2 r = __builtin_amdgcn_permlane32_swap(__float_as_uint(x), __float_as_uint(x), false, false); y = __uint_as_float(r[1]); return __uint_as_float(r[0]); }
DEV float x16(float x, float& y) { u32x2 r = __builtin_amdgcn_permlane16_swap(__float_as_uint(x), __float_as_uint(x), false, false); y = __uint_as_float(r[1]); return __uint_as_float(r[0]); }
DEV float rows_max(float x) { float y; x = x32(x, y); x = fmaxf(x, y); x = x16(x, y); return fmaxf(x, y); }
DEV float rows_sum(float x) { float y; x = x32(x, y); x += y; x = x16(x, y); return x + y; }
DEV float sum8(float x) { x += __int_as_float(__builtin_amdgcn_update_dpp(0, __float_as_int(x), 0x128, 0xf, 0xf, false)); return rows_sum(x); }

__global__ __launch_bounds__(512) void prep_kernel(const uint16_t* __restrict__ x, const uint16_t* __restrict__ wqkv, const uint16_t* __restrict__ wo,
                                                   const uint16_t* __restrict__ w1, const uint16_t* __restrict__ w3, const uint16_t* __restrict__ w2,
                                                   char* __restrict__ ws) {
  __shared__ float red[56][33];
  const int b = blockIdx.x, t = threadIdx.x;
  if (b >= 224) {
    const int wv = t >> 6, l = t & 63, mt = (b - 224) * 8 + wv;
    if (b == 224) { for (int i = t; i < 2048; i += 512) ((u32x4*)(ws + O_CNT))[i] = u32x4{0, 0, 0, 0}; }
    const int u = mt * 16 + (l & 15), tokn = 2 * (u & 1023) + (u >> 10);
    u32x4 v[14];
#pragma unroll
    for (int k = 0; k < 14; ++k) v[k] = *(const u32x4*)(x + (size_t)tokn * 448 + k * 32 + (l >> 4) * 8);
    float s = 0.f;
#pragma unroll
    for (int k = 0; k < 14; ++k) {
      *(u32x4*)(ws + O_X + (mt * 14 + k) * 1024 + l * 16) = v[k];
#pragma unroll
      for (int j = 0; j < 4; ++j) s += blo(v[k][j]) + bhi(v[k][j]);
    }
    const float mean = rows_sum(s) * (1.f / 448.f);
    float m2 = 0.f;
#pragma unroll
    for (int k = 0; k < 14; ++k)
#pragma unroll
      for (int j = 0; j < 4; ++j) { float a = blo(v[k][j]) - mean, c = bhi(v[k][j]) - mean; m2 += a * a + c * c; }
    m2 = rows_sum(m2) * 0.25f;
    *(f32x2*)(ws + O_ST + u * 32 + (l >> 4) * 8) = f32x2{mean, m2};
    return;
  }
  const int cq = t & 7, ch = t >> 3;
  const uint16_t* src; int ld, n0, kc = ch, kind;
  if (b < 42) { kind = 0; src = wqkv; ld = 1344; n0 = b * 32; }
  else if (b < 98) { kind = 1; src = w1; ld = 1792; n0 = (b - 42) * 32; }
  else if (b < 154) { kind = 2; src = w3; ld = 1792; n0 = (b - 98) * 32; }
  else if (b < 168) { kind = 3; src = wo; ld = 448; n0 = (b - 154) * 32; }
  else { kind = 4; src = w2; ld = 448; n0 = ((b - 168) % 14) * 32; kc = ch + ((b - 168) / 14) * 56; }
  const int n = n0 + cq * 4;
  const bool act = (kind == 3) || ch < 56;
  float cs[4] = {0.f, 0.f, 0.f, 0.f};
  unsigned csoff = 0, rstep2 = 2;
  if (act) {
    u32x2 v[8];
    const int q = kc & 3, ks = kc >> 2;
#pragma unroll
    for (int j = 0; j < 8; ++j) {
      int row = kc * 8 + j; bool ok = true;
      if (kind == 3) { int f = 16 * (j >> 2) + 4 * q + (j & 3); ok = f < 28; row = ks * 28 + (ok ? f : 0); }
      v[j] = *(const u32x2*)(src + (size_t)row * ld + n);
      if (!ok) v[j] = u32x2{0, 0};
    }
    unsigned dst; int r0; bool padz = false;
    if (kind == 0) { int part = n / 448, nn = n % 448, hd = nn / 28, f = nn % 28, nt = 2 * part + (f >> 4);
      r0 = f & 15; dst = O_WQ + ((hd * 14 + ks) * 6 + nt) * 1024; csoff = O_CSQ + (hd * 96 + nt * 16) * 4; padz = (f == 24); }
    else if (kind <= 2) { int c = n / 112, nt = (n % 112) >> 3; r0 = ((n & 4) ? 8 : 0) + (kind == 2 ? 2 : 0); rstep2 = 4;
      dst = O_W13 + ((c * 14 + ks) * 14 + nt) * 1024; csoff = O_CS13 + (c * 224 + nt * 16) * 4; }
    else { int cg = n / 112, nt = (n % 112) >> 4; r0 = n & 15;
      dst = (kind == 3) ? O_WO + ((cg * 16 + ks) * 7 + nt) * 1024 : O_W2 + ((cg * 56 + ks) * 7 + nt) * 1024; }
    dst += q * 256; csoff += r0 * 4;
#pragma unroll
    for (int i = 0; i < 4; ++i) {
      u32x4 g;
#pragma unroll
      for (int j = 0; j < 4; ++j) {
        unsigned a = v[2 * j][i >> 1], c = v[2 * j + 1][i >> 1];
        g[j] = (i & 1) ? __builtin_amdgcn_perm(c, a, 0x07060302u) : __builtin_amdgcn_perm(c, a, 0x05040100u);
        cs[i] += blo(g[j]) + bhi(g[j]);
      }
      *(u32x4*)(ws + dst + (r0 + (i & 1) + (i >> 1) * rstep2) * 16) = g;
      if (padz) *(u32x4*)(ws + dst + (12 + i) * 16) = u32x4{0, 0, 0, 0};
    }
    if (padz && ch == 0) *(f32x4*)(ws + csoff + 16) = f32x4{0.f, 0.f, 0.f, 0.f};
  }
  if (kind <= 2) {
    if (act) { for (int i = 0; i < 4; ++i) red[ch][cq * 4 + i] = cs[i]; }
    __syncthreads();
    if (ch < 4) {
      float s = 0.f;
      for (int k = 0; k < 56; ++k) s += red[k][cq * 4 + ch];
      *(float*)(ws + csoff + ((ch & 1) + (ch >> 1) * rstep2) * 4) = s;
    }
  }
}

struct Ctx { rsrc_t ws; gu32* cnt; char* sm; int w, l, g, c; unsigned v16;
  DEV void fresh() { asm volatile("" : "+v"(l)); v16 = l * 16; } };
#define SB() __builtin_amdgcn_sched_barrier(0)

DEV void publish(const Ctx& C, int idx) {
  wait_vm<0>(); bar();
  if (threadIdx.x == 0) __hip_atomic_fetch_add(C.cnt + (idx * 16 + C.g) * 64, 1u, __ATOMIC_RELAXED, __HIP_MEMORY_SCOPE_AGENT);
}
DEV void poll(gu32* p) {
#pragma unroll 1
  for (unsigned s = 0; s < 400000u; ++s) {
    if ((unsigned)__builtin_amdgcn_readfirstlane(__hip_atomic_load(p, __ATOMIC_RELAXED, __HIP_MEMORY_SCOPE_AGENT)) >= 16u) break;
    __builtin_amdgcn_s_sleep(1);
  }
}
DEV void await(const Ctx& C, int idx, bool halo) {
  if (C.w == 0) { poll(C.cnt + (idx * 16 + C.g) * 64); if (halo && (C.g & 7)) poll(C.cnt + (idx * 16 + C.g - 1) * 64); }
  bar();
}
DEV void stats_to_lds(const Ctx& C, u32x4 a, u32x4 b, int slot) {
  float m0 = __uint_as_float(a[0]), m1 = __uint_as_float(a[2]), m2 = __uint_as_float(b[0]), m3 = __uint_as_float(b[2]);
  float mean = 0.25f * ((m0 + m1) + (m2 + m3));
  m0 -= mean; m1 -= mean; m2 -= mean; m3 -= mean;
  float M2 = (__uint_as_float(a[1]) + __uint_as_float(a[3])) + (__uint_as_float(b[1]) + __uint_as_float(b[3])) + 112.f * ((m0 * m0 + m1 * m1) + (m2 * m2 + m3 * m3));
  float rstd = __builtin_amdgcn_rsqf(M2 * (1.f / 448.f) + 1e-5f);
  *(f32x2*)(C.sm + L_ST + slot * 8) = f32x2{mean, rstd};
}
DEV void wq_dma(const Ctx& C) {
  const unsigned gb = opq(O_WQ + (C.c * 84 + C.w * 21) * 1024), lb = opq(C.w * 21 * 1024);
  sfor<0, 21>([&](auto I) { dma16<false>(C.ws, C.v16, gb + I.v * 1024, lb + I.v * 1024); });
}
DEV void attend(const Ctx& C, u32x4 Q, u32x4 Kp, u32x4 Vp, u32x4 Ko, u32x4 Vo, bool hasprev, unsigned dst) {
  const f32x4 z = {0.f, 0.f, 0.f, 0.f};
  f32x4 s0 = mfma(Kp, Q, z), s1 = mfma(Ko, Q, z);
  const int d = (C.l & 15) - 4 * (C.l >> 4);
  float m = -3e38f;
#pragma unroll
  for (int i = 0; i < 4; ++i) {
    s0[i] = (hasprev && d - i + 16 <= 7) ? s0[i] : -3e38f;
    s1[i] = ((unsigned)(d - i) <= 7u) ? s1[i] : -3e38f;
    m = fmaxf(m, fmaxf(s0[i], s1[i]));
  }
  m = rows_max(m);
  const float sc = 0.18898223650461362f * 1.4426950408889634f;
  float sum = 0.f;
#pragma unroll
  for (int i = 0; i < 4; ++i) {
    s0[i] = __builtin_amdgcn_exp2f((s0[i] - m) * sc); s1[i] = __builtin_amdgcn_exp2f((s1[i] - m) * sc);
    sum += s0[i] + s1[i];
  }
  const float r = __builtin_amdgcn_rcpf(rows_sum(sum));
  u32x4 P = {pk(s0[0] * r, s0[1] * r), pk(s0[2] * r, s0[3] * r), pk(s1[0] * r, s1[1] * r), pk(s1[2] * r, s1[3] * r)};
  f32x4 o0 = mfma(u32x4{Vp[0], Vp[1], Vo[0], Vo[1]}, P, z), o1 = mfma(u32x4{Vp[2], Vp[3], Vo[2], Vo[3]}, P, z);
  st16<16>(u32x4{pk(o0[0], o0[1]), pk(o0[2], o0[3]), pk(o1[0], o1[1]), pk(o1[2], o1[3])}, C.ws, C.v16, dst);
}
DEV u32x2 normT(f32x4 a, f32x4 cs, f32x2 st) {
  return u32x2{pk(st[1] * (a[0] - st[0] * cs[0]), st[1] * (a[1] - st[0] * cs[1])), pk(st[1] * (a[2] - st[0] * cs[2]), st[1] * (a[3] - st[0] * cs[3]))};
}
DEV u32x2 normF(f32x4 a, float cs, f32x4 s01, f32x4 s23) {
  return u32x2{pk(s01[1] * (a[0] - s01[0] * cs), s01[3] * (a[1] - s01[2] * cs)), pk(s23[1] * (a[2] - s23[0] * cs), s23[3] * (a[3] - s23[2] * cs))};
}
template <bool FINAL> DEV void ksplit_epilogue(const Ctx& C, f32x4 (&acc)[2][7], u32x4 (&res)[2], unsigned xdst, unsigned stdst, rsrc_t out) {
  const int q = C.l >> 4, tok = C.l & 15;
  sfor<0, 14>([&](auto I) { constexpr int m = I.v / 7, nt = I.v % 7;
    *(f32x4*)(C.sm + L_RED + (C.w * 14 + I.v) * 1024 + q * 256 + ((tok + 8 * (nt & 1)) & 15) * 16) = acc[m][nt]; });
  bar();
  const int g8 = C.l >> 3, m = C.w >> 1, tk = 8 * (C.w & 1) + (C.l & 7), cg = C.c >> 2, mt = C.g * 8 + (C.c & 3) * 2 + m;
  float val[2][8]; float s = 0.f;
#pragma unroll
  for (int h = 0; h < 2; ++h) {
    const int gr = min(g8 + 8 * h, 13), nt = gr >> 1;
    const char* p = C.sm + L_RED + (m * 7 + nt) * 1024 + (gr & 1) * 512 + ((tk + 8 * (nt & 1)) & 15) * 16;
    f32x4 a = *(const f32x4*)p, b = *(const f32x4*)(p + 256);
#pragma unroll
    for (int k = 1; k < 4; ++k) { a += *(const f32x4*)(p + k * 14336); b += *(const f32x4*)(p + k * 14336 + 256); }
    u32x4 r = res[h];
    r = u32x4{pk(a[0] + blo(r[0]), a[1] + bhi(r[0])), pk(a[2] + blo(r[1]), a[3] + bhi(r[1])), pk(b[0] + blo(r[2]), b[1] + bhi(r[2])), pk(b[2] + blo(r[3]), b[3] + bhi(r[3]))};
    res[h] = r;
    const bool ok = h == 0 || g8 < 6;
    const int G = cg * 14 + gr;
    if constexpr (FINAL) { const int u = mt * 16 + tk; if (ok) st16<0>(r, out, (2 * (u & 1023) + (u >> 10)) * 896 + G * 16, 0); }
    else {
      if (ok) st16<16>(r, C.ws, (mt * 14 + (G >> 2)) * 1024 + (G & 3) * 256 + tk * 16, xdst);
#pragma unroll
      for (int j = 0; j < 4; ++j) { val[h][2 * j] = blo(r[j]); val[h][2 * j + 1] = bhi(r[j]); s += ok ? val[h][2 * j] + val[h][2 * j + 1] : 0.f; }
    }
  }
  if constexpr (!FINAL) {
    const float mean = sum8(s) * (1.f / 112.f); float m2 = 0.f, m2b = 0.f;
#pragma unroll
    for (int j = 0; j < 8; ++j) { float a = val[0][j] - mean, b = val[1][j] - mean; m2 += a * a; m2b += b * b; }
    m2 = sum8(g8 < 6 ? m2 + m2b : m2);
    if (g8 == 0) __builtin_amdgcn_raw_buffer_store_b64(u32x2{__float_as_uint(mean), __float_as_uint(m2)}, C.ws, (mt * 16 + tk) * 32 + cg * 8, stdst, 16);
  }
}

__global__ __launch_bounds__(256, 1) void main_kernel(char* wsp, uint16_t* outp) {
  __shared__ __attribute__((aligned(1024))) char sm[L_END];
  Ctx C;
  C.ws = __builtin_amdgcn_make_buffer_rsrc(wsp, 0, WS_END, 0x00020000);
  const rsrc_t out = __builtin_amdgcn_make_buffer_rsrc(outp, 0, 2048 * 448 * 2, 0x00020000);
  C.cnt = (gu32*)wsp; C.sm = sm;
  C.w = __builtin_amdgcn_readfirstlane(threadIdx.x >> 6); C.l = threadIdx.x & 63; C.v16 = C.l * 16;
  { const int b = blockIdx.x, x = b & 7, s = b >> 3;
    if (XMAP == 0) { C.g = 2 * x + (s >> 4); C.c = s & 15; } else { C.g = 4 * (x >> 1) + (s >> 3); C.c = (x & 1) * 8 + (s & 7); } }
  const int w = C.w, g = C.g, c = C.c, cg = c >> 2;
  const f32x4 z4 = {0.f, 0.f, 0.f, 0.f};
  const unsigned mp0 = g * 8 + (c & 3) * 2;
  wq_dma(C);
  u32x4 res[2];
#pragma unroll
  for (int h = 0; h < 2; ++h) { const int l = C.l, G = cg * 14 + min((l >> 3) + 8 * h, 13);
    res[h] = ld16<0>(C.ws, ((mp0 + (w >> 1)) * 14 + (G >> 2)) * 1024 + (G & 3) * 256 + (8 * (w & 1) + (l & 7)) * 16, O_X); }

#pragma unroll 1
  for (int L = 0; L < 2; ++L) {
    u32x4 wo[28], w13[56], w2[98];
    {
      C.fresh(); const int q = C.l >> 4, tok = C.l & 15, t = w * 64 + C.l;
      const unsigned Xin = O_X + 2 * L * X_SZ, Sin = O_ST + 2 * L * ST_SZ;
      const f32x4 csq0 = __builtin_bit_cast(f32x4, ld16<0>(C.ws, q * 16, O_CSQ + c * 384)), csq1 = __builtin_bit_cast(f32x4, ld16<0>(C.ws, q * 16, O_CSQ + c * 384 + 64));
      const f32x4 csk0 = __builtin_bit_cast(f32x4, ld16<0>(C.ws, q * 16, O_CSQ + c * 384 + 128)), csk1 = __builtin_bit_cast(f32x4, ld16<0>(C.ws, q * 16, O_CSQ + c * 384 + 192));
      const float csv0 = __uint_as_float(__builtin_amdgcn_raw_buffer_load_b32(C.ws, tok * 4, O_CSQ + c * 384 + 256, 0));
      const float csv1 = __uint_as_float(__builtin_amdgcn_raw_buffer_load_b32(C.ws, tok * 4, O_CSQ + c * 384 + 320, 0));
      wait_vm<0>();
      if (L) await(C, 3, true); else bar();
      const unsigned mh = (g & 7) ? 8 * g - 1 : 8 * g, ma = 8 * g + 2 * w;
      const int sl = min(t, 143); const unsigned su = sl < 16 ? mh * 16 + sl : g * 128 + sl - 16;
      u32x4 sa = ld16<16>(C.ws, su * 32, Sin), sb = ld16<16>(C.ws, su * 32 + 16, Sin);
      u32x4 xh[14], xa[14], xb[14];
      const unsigned hb = opq(Xin + mh * 14336), ab_ = opq(Xin + ma * 14336), wob = opq(O_WO + (cg * 16 + w * 4) * 7168);
      sfor<0, 14>([&](auto I) { xh[I.v] = ldf<16, I.v>(C.ws, C.v16, hb); });
      sfor<0, 14>([&](auto I) { xa[I.v] = ldf<16, I.v>(C.ws, C.v16, ab_); xb[I.v] = ldf<16, 14 + I.v>(C.ws, C.v16, ab_); });
      __builtin_amdgcn_sched_barrier(0);
      stats_to_lds(C, sa, sb, sl);
      f32x4 ah = z4;
      const char* wl = sm + C.v16;
      { u32x4 Wh[14];
        sfor<0, 14>([&](auto I) { Wh[I.v] = *(const u32x4*)(wl + (I.v * 6 + 2 + w) * 1024); });
        if (w < 2) sfor<0, 14>([&](auto I) { ah = mfma(Wh[I.v], xh[I.v], ah); });
        else sfor<0, 14>([&](auto I) { ah = mfma(xh[I.v], Wh[I.v], ah); }); }
      f32x4 aa[6], ab[6];
      sfor<0, 6>([&](auto N) { aa[N.v] = z4; ab[N.v] = z4; });
      constexpr int D1 = 5; u32x4 W[84];
      sfor<0, D1>([&](auto I) { W[I.v] = *(const u32x4*)(wl + I.v * 1024); });
      SB();
      sfor<0, 84>([&](auto I) { constexpr int k = I.v / 6, n = I.v % 6;
        if constexpr (I.v + D1 < 84) W[I.v + D1] = *(const u32x4*)(wl + (I.v + D1) * 1024);
        if constexpr (n == 1 || n == 4) wo[2 * k + n / 4] = ldf<0, 2 * k + n / 4>(C.ws, C.v16, wob);
        if constexpr (n < 4) { aa[n] = mfma(W[I.v], xa[k], aa[n]); ab[n] = mfma(W[I.v], xb[k], ab[n]); }
        else { aa[n] = mfma(xa[k], W[I.v], aa[n]); ab[n] = mfma(xb[k], W[I.v], ab[n]); }
        SB(); });
      bar();
      const char* sp = sm + L_ST;
      { u32x2 hv;
        if (w < 2) hv = normT(ah, w ? csk1 : csk0, *(const f32x2*)(sp + tok * 8));
        else hv = normF(ah, w == 2 ? csv0 : csv1, *(const f32x4*)(sp + q * 32), *(const f32x4*)(sp + q * 32 + 16));
        *(u32x2*)(sm + L_EX + (w >> 1) * 1024 + C.v16 + (w & 1) * 8) = hv; }
      auto cat = [](u32x2 a, u32x2 b) { return u32x4{a[0], a[1], b[0], b[1]}; };
      const char* spb = sp + (32 + 32 * w) * 8; const char* spa = spb - 128;
      const f32x2 tb = *(const f32x2*)(spb + tok * 8), ta = *(const f32x2*)(spa + tok * 8);
      const f32x4 b01 = *(const f32x4*)(spb + q * 32), b23 = *(const f32x4*)(spb + q * 32 + 16), a01 = *(const f32x4*)(spa + q * 32), a23 = *(const f32x4*)(spa + q * 32 + 16);
      const u32x4 Kb = cat(normT(ab[2], csk0, tb), normT(ab[3], csk1, tb)), Vb = cat(normF(ab[4], csv0, b01, b23), normF(ab[5], csv1, b01, b23));
      *(u32x4*)(sm + L_EX + (w + 1) * 2048 + C.v16) = Kb; *(u32x4*)(sm + L_EX + (w + 1) * 2048 + 1024 + C.v16) = Vb;
      const u32x4 Ka = cat(normT(aa[2], csk0, ta), normT(aa[3], csk1, ta)), Va = cat(normF(aa[4], csv0, a01, a23), normF(aa[5], csv1, a01, a23));
      const u32x4 Qa = cat(normT(aa[0], csq0, ta), normT(aa[1], csq1, ta)), Qb = cat(normT(ab[0], csq0, tb), normT(ab[1], csq1, tb));
      const unsigned od = O_O + L * O_SZ + (ma * 16 + c) * 1024;
      attend(C, Qb, Ka, Va, Kb, Vb, true, od + 16384);
      bar();
      const u32x4 Kp = *(const u32x4*)(sm + L_EX + w * 2048 + C.v16), Vp = *(const u32x4*)(sm + L_EX + w * 2048 + 1024 + C.v16);
      attend(C, Qa, Kp, Vp, Ka, Va, w > 0 || (g & 7), od);
      publish(C, L * 4 + 0);
    }
    const unsigned b13 = opq(O_W13 + (c * 196 + 3 * w) * 1024), b13x = opq(O_W13 + (c * 196 + 12 + (w >> 1)) * 1024), b2 = opq(O_W2 + (cg * 56 + w * 14) * 7168);
    auto ld13 = [&](auto I) { constexpr int ks = I.v / 4, j = I.v % 4;
      w13[I.v] = ld16<0>(C.ws, C.v16 + (j < 3 ? j * 1024 : 0), (j < 3 ? b13 : b13x) + ks * 14336); };
    auto ld2 = [&](auto I) { w2[I.v] = ldf<0, I.v>(C.ws, C.v16, b2); };
    {
      C.fresh();
      sfor<0, N13E>(ld13);
      await(C, L * 4 + 0, false);
      u32x4 of[2][4];
      const unsigned ob = opq(O_O + L * O_SZ + (mp0 * 16 + w * 4) * 1024);
      sfor<0, 8>([&](auto I) { of[I.v & 1][I.v >> 1] = ldf<16, (I.v & 1) * 16 + (I.v >> 1)>(C.ws, C.v16, ob); });
      __builtin_amdgcn_sched_barrier(0);
      f32x4 acc[2][7];
      sfor<0, 14>([&](auto I) { acc[I.v / 7][I.v % 7] = z4; });
      sfor<0, 4>([&](auto K) { sfor<0, 7>([&](auto N) { acc[0][N.v] = mfma(wo[K.v * 7 + N.v], of[0][K.v], acc[0][N.v]); acc[1][N.v] = mfma(wo[K.v * 7 + N.v], of[1][K.v], acc[1][N.v]); }); });
      C.fresh();
      ksplit_epilogue<false>(C, acc, res, O_X + (2 * L + 1) * X_SZ, O_ST + (2 * L + 1) * ST_SZ, out);
      publish(C, L * 4 + 1);
    }
    {
      C.fresh();
      sfor<N13E, 56>(ld13);
      await(C, L * 4 + 1, false);
      const unsigned Xin = O_X + (2 * L + 1) * X_SZ, Sin = O_ST + (2 * L + 1) * ST_SZ;
      const int sl = (w & 1) * 64 + C.l; const unsigned su = g * 128 + sl;
      u32x4 sa = ld16<16>(C.ws, su * 32, Sin), sb = ld16<16>(C.ws, su * 32 + 16, Sin);
      const unsigned xg = opq(Xin + (g * 8 + 2 * w) * 14336), xs = opq(w * 2048);
      sfor<0, 28>([&](auto I) { constexpr int ks = I.v >> 1, m = I.v & 1; dma16<true>(C.ws, C.v16, xg + (m * 14 + ks) * 1024, xs + (ks * 8 + m) * 1024); });
      f32x4 acc[8][3], ax[4], cs[4];
      sfor<0, 8>([&](auto M) { acc[M.v][0] = z4; acc[M.v][1] = z4; acc[M.v][2] = z4; ax[M.v & 3] = z4; });
      const char* x0 = sm + C.v16 + (w & 1) * 4096; const char* x1 = sm + C.v16 + ((w & 1) ^ 1) * 4096 - 4096;
      constexpr int D3 = 4; u32x4 X[112];
      auto rdx = [&](auto I) { X[I.v] = *(const u32x4*)(((I.v & 4) ? x1 : x0) + I.v * 1024); };
      wait_vm<26>(); stats_to_lds(C, sa, sb, sl); bar();
      sfor<0, D3>(rdx);
      SB();
      sfor<0, 112>([&](auto I) { constexpr int k = I.v / 8, m = I.v % 8, nx = I.v + D3;
        if constexpr (nx == 8) { wait_vm<20>(); bar(); }
        if constexpr (nx == 32) { wait_vm<3 * PF3>(); bar(); }
        if constexpr (nx < 112) rdx(IC<nx>{});
        sfor<0, 3>([&](auto J) { acc[m][J.v] = mfma(w13[k * 4 + J.v], X[I.v], acc[m][J.v]); });
        if constexpr (m < 4) ax[m] = mfma(w13[k * 4 + 3], X[I.v], ax[m]);
        if constexpr (k > 0 && m < PF3) ld2(IC<(k - 1) * PF3 + m>{});
        if constexpr (k == 12 && m >= 4) cs[m - 4] = __builtin_bit_cast(f32x4, ld16<0>(C.ws, (C.l >> 4) * 16, O_CS13 + (c * 224 + (m < 7 ? 3 * w + m - 4 : 12 + (w >> 1)) * 16) * 4));
        SB(); });
      C.fresh(); const int q = C.l >> 4, tok = C.l & 15, mx = 4 * (w & 1);
      const unsigned Hd = O_H + L * H_SZ;
      auto unit = [&](f32x4 a0, f32x4 a1, f32x4 a2, f32x4 a3, f32x4 csn, int mb, int nt) {
        f32x4 a[4] = {a0, a1, a2, a3}; unsigned hv[4];
#pragma unroll
        for (int i = 0; i < 4; ++i) {
          const f32x2 st = *(const f32x2*)(sm + L_ST + ((mb + i) * 16 + tok) * 8);
          float g0 = st[1] * (a[i][0] - st[0] * csn[0]), g1 = st[1] * (a[i][1] - st[0] * csn[1]);
          float u0 = st[1] * (a[i][2] - st[0] * csn[2]), u1 = st[1] * (a[i][3] - st[0] * csn[3]);
          float e0 = __builtin_amdgcn_exp2f(g0 * -1.4426950408889634f), e1 = __builtin_amdgcn_exp2f(g1 * -1.4426950408889634f);
          hv[i] = pk(g0 * u0 * __builtin_amdgcn_rcpf(1.f + e0), g1 * u1 * __builtin_amdgcn_rcpf(1.f + e1));
        }
        u32x2 ac = __builtin_amdgcn_permlane32_swap(hv[0], hv[2], false, false), bd = __builtin_amdgcn_permlane32_swap(hv[1], hv[3], false, false);
        u32x2 y01 = __builtin_amdgcn_permlane16_swap(ac[0], bd[0], false, false), y23 = __builtin_amdgcn_permlane16_swap(ac[1], bd[1], false, false);
        const unsigned G = 14 * c + nt;
        st16<16>(u32x4{y01[0], y01[1], y23[0], y23[1]}, C.ws, q * 57344 + tok * 16, Hd + ((g * 8 + mb) * 56 + (G >> 2)) * 1024 + (G & 3) * 256);
      };
      sfor<0, 6>([&](auto U) { constexpr int j = U.v >> 1, mb = 4 * (U.v & 1); unit(acc[mb][j], acc[mb + 1][j], acc[mb + 2][j], acc[mb + 3][j], cs[j], mb ^ mx, 3 * w + j); SB(); });
      unit(ax[0], ax[1], ax[2], ax[3], cs[3], mx, 12 + (w >> 1));
      publish(C, L * 4 + 2);
    }
    {
      C.fresh();
      constexpr int N2 = 13 * PF3 + N2B;
      sfor<13 * PF3, N2>(ld2);
      await(C, L * 4 + 2, false);
      const unsigned Hs = opq(O_H + L * H_SZ + (mp0 * 56 + w * 14) * 1024);
      u32x4 hf[2][14];
      auto ldh = [&](auto K) { hf[0][K.v] = ldf<16, K.v>(C.ws, C.v16, Hs); hf[1][K.v] = ldf<16, 56 + K.v>(C.ws, C.v16, Hs); };
      sfor<0, NH0>(ldh);
      SB();
      f32x4 acc[2][7];
      sfor<0, 14>([&](auto I) { acc[I.v / 7][I.v % 7] = z4; });
      sfor<0, 14>([&](auto K) {
        sfor<0, 7>([&](auto N) { acc[0][N.v] = mfma(w2[K.v * 7 + N.v], hf[0][K.v], acc[0][N.v]); acc[1][N.v] = mfma(w2[K.v * 7 + N.v], hf[1][K.v], acc[1][N.v]);
          if constexpr (N2 + 7 * K.v + N.v < 98) ld2(IC<N2 + 7 * K.v + N.v>{});
          SB(); });
        if constexpr (K.v + NH0 < 14) ldh(IC<K.v + NH0>{});
      });
      C.fresh();
      if (L == 0) { ksplit_epilogue<false>(C, acc, res, O_X + 2 * X_SZ, O_ST + 2 * ST_SZ, out); publish(C, 3); wq_dma(C); }
      else ksplit_epilogue<true>(C, acc, res, 0, 0, out);
    }
  }
}

extern "C" void kernel_launch(void* const* d_in, const int* in_sizes, int n_in, void* d_out, int out_size, void* d_ws, size_t ws_size,
                              hipStream_t stream) {
  prep_kernel<<<240, 512, 0, stream>>>((const uint16_t*)d_in[0], (const uint16_t*)d_in[1], (const uint16_t*)d_in[2], (const uint16_t*)d_in[4],
                                       (const uint16_t*)d_in[5], (const uint16_t*)d_in[6], (char*)d_ws);
  main_kernel<<<256, 256, 0, stream>>>((char*)d_ws, (uint16_t*)d_out);
}
```

```cpp
#include <hip/hip_runtime.h>
#include <hip/hip_bf16.h>
#include <stdint.h>

typedef __attribute__((ext_vector_type(8))) __bf16 bf16x8;
typedef __attribute__((ext_vector_type(2))) __bf16 bf16x2;
typedef __attribute__((ext_vector_type(4))) float f32x4;
typedef __attribute__((ext_vector_type(2))) float f32x2;
typedef __attribute__((ext_vector_type(4))) unsigned u32x4;
typedef __attribute__((ext_vector_type(2))) unsigned u32x2;
typedef __attribute__((address_space(1))) unsigned gu32;
typedef __amdgpu_buffer_rsrc_t rsrc_t;

#define XMAP 1
#define CMAP 1
#define ROLE_CG(c) (CMAP ? (c) & 3 : (c) >> 2)
#define ROLE_MP(c) (CMAP ? (c) >> 2 : (c) & 3)
#define N13A 16
#define N13B 2
#define NX1 5
#define N2A 2
#define PF3 4
#define ND2 3
#define NH0 6

constexpr unsigned O_CNT = 0;
constexpr unsigned O_CSQ = 32768;
constexpr unsigned O_CS13 = O_CSQ + 6144;
constexpr unsigned O_ST = 65536;
constexpr unsigned ST_SZ = 65536;
constexpr unsigned O_WQ = O_ST + 4 * ST_SZ;
constexpr unsigned O_WO = O_WQ + 16 * 14 * 6 * 1024;
constexpr unsigned O_W13 = O_WO + 4 * 16 * 7 * 1024;
constexpr unsigned O_W2 = O_W13 + 16 * 14 * 14 * 1024;
constexpr unsigned O_X = O_W2 + 4 * 56 * 7 * 1024;
constexpr unsigned X_SZ = 128 * 14 * 1024;
constexpr unsigned O_O = O_X + 4 * X_SZ;
constexpr unsigned O_SZ = 128 * 16 * 1024;
constexpr unsigned O_H = O_O + 2 * O_SZ;
constexpr unsigned H_SZ = 128 * 56 * 1024;
constexpr unsigned WS_END = O_H + 2 * H_SZ;
constexpr unsigned L_RED = 86016, L_ST = 143360, L_EX = 144640, L_END = L_EX + 10240;

#define DEV __device__ __forceinline__
template <int N> struct IC { static constexpr int v = N; };
template <int B, int E, class F> DEV void sfor(F&& f) { if constexpr (B < E) { f(IC<B>{}); sfor<B + 1, E>(f); } }

DEV f32x4 mfma(u32x4 a, u32x4 b, f32x4 c) {
  return __builtin_amdgcn_mfma_f32_16x16x32_bf16(__builtin_bit_cast(bf16x8, a), __builtin_bit_cast(bf16x8, b), c, 0, 0, 0);
}
DEV unsigned pk(float lo, float hi) { f32x2 v = {lo, hi}; return __builtin_bit_cast(unsigned, __builtin_convertvector(v, bf16x2)); }
DEV float blo(unsigned u) { return __uint_as_float(u << 16); }
DEV float bhi(unsigned u) { return __uint_as_float(u & 0xffff0000u); }
template <int AUX> DEV u32x4 ld16(rsrc_t r, unsigned voff, unsigned soff) { return __builtin_amdgcn_raw_buffer_load_b128(r, voff, soff, AUX); }
template <int AUX> DEV void st16(u32x4 v, rsrc_t r, unsigned voff, unsigned soff) { __builtin_amdgcn_raw_buffer_store_b128(v, r, voff, soff, AUX); }
DEV unsigned opq(unsigned x) { x = __builtin_amdgcn_readfirstlane(x); asm volatile("" : "+s"(x)); return x; }
template <int AUX, int F> DEV u32x4 ldf(rsrc_t r, unsigned v16, unsigned base) { return ld16<AUX>(r, v16 + (F & 3) * 1024, base + (F >> 2) * 4096); }
template <bool SC1> DEV void dma16(rsrc_t r, unsigned voff, unsigned soff, unsigned lds) {
  if constexpr (SC1) asm volatile("s_mov_b32 m0, %0\n\ts_nop 0\n\tbuffer_load_dwordx4 %1, %2, %3 offen sc1 lds" :: "s"(lds), "v"(voff), "s"(r), "s"(soff) : "memory");
  else asm volatile("s_mov_b32 m0, %0\n\ts_nop 0\n\tbuffer_load_dwordx4 %1, %2, %3 offen lds" :: "s"(lds), "v"(voff), "s"(r), "s"(soff) : "memory");
}
template <int N> DEV void wait_vm() { asm volatile("s_waitcnt vmcnt(%0)" :: "i"(N > 63 ? 63 : N) : "memory"); }
DEV void bar() { asm volatile("s_waitcnt lgkmcnt(0)\n\ts_barrier" ::: "memory"); }
DEV float x32(float x, float& y) { u32x2 r = __builtin_amdgcn_permlane32_swap(__float_as_uint(x), __float_as_uint(x), false, false); y = __uint_as_float(r[1]); return __uint_as_float(r[0]); }
DEV float x16(float x, float& y) { u32x2 r = __builtin_amdgcn_permlane16_swap(__float_as_uint(x), __float_as_uint(x), false, false); y = __uint_as_float(r[1]); return __uint_as_float(r[0]); }
DEV float rows_max(float x) { float y; x = x32(x, y); x = fmaxf(x, y); x = x16(x, y); return fmaxf(x, y); }
DEV float rows_sum(float x) { float y; x = x32(x, y); x += y; x = x16(x, y); return x + y; }
DEV float sum8(float x) { x += __int_as_float(__builtin_amdgcn_update_dpp(0, __float_as_int(x), 0x128, 0xf, 0xf, false)); return rows_sum(x); }

__global__ __launch_bounds__(512) void prep_kernel(const uint16_t* __restrict__ x, const uint16_t* __restrict__ wqkv, const uint16_t* __restrict__ wo,
                                                   const uint16_t* __restrict__ w1, const uint16_t* __restrict__ w3, const uint16_t* __restrict__ w2,
                                                   char* __restrict__ ws) {
  __shared__ float red[56][33];
  const int b = blockIdx.x, t = threadIdx.x;
  if (b >= 224) {
    const int wv = t >> 6, l = t & 63, mt = (b - 224) * 8 + wv;
    if (b == 224) { for (int i = t; i < 2048; i += 512) ((u32x4*)(ws + O_CNT))[i] = u32x4{0, 0, 0, 0}; }
    const int u = mt * 16 + (l & 15), tokn = 2 * (u & 1023) + (u >> 10);
    u32x4 v[14];
#pragma unroll
    for (int k = 0; k < 14; ++k) v[k] = *(const u32x4*)(x + (size_t)tokn * 448 + k * 32 + (l >> 4) * 8);
    float s = 0.f;
#pragma unroll
    for (int k = 0; k < 14; ++k) {
      *(u32x4*)(ws + O_X + (mt * 14 + k) * 1024 + l * 16) = v[k];
#pragma unroll
      for (int j = 0; j < 4; ++j) s += blo(v[k][j]) + bhi(v[k][j]);
    }
    const float mean = rows_sum(s) * (1.f / 448.f);
    float m2 = 0.f;
#pragma unroll
    for (int k = 0; k < 14; ++k)
#pragma unroll
      for (int j = 0; j < 4; ++j) { float a = blo(v[k][j]) - mean, c = bhi(v[k][j]) - mean; m2 += a * a + c * c; }
    m2 = rows_sum(m2) * 0.25f;
    *(f32x2*)(ws + O_ST + u * 32 + (l >> 4) * 8) = f32x2{mean, m2};
    return;
  }
  const int cq = t & 7, ch = t >> 3;
  const uint16_t* src; int ld, n0, kc = ch, kind;
  if (b < 42) { kind = 0; src = wqkv; ld = 1344; n0 = b * 32; }
  else if (b < 98) { kind = 1; src = w1; ld = 1792; n0 = (b - 42) * 32; }
  else if (b < 154) { kind = 2; src = w3; ld = 1792; n0 = (b - 98) * 32; }
  else if (b < 168) { kind = 3; src = wo; ld = 448; n0 = (b - 154) * 32; }
  else { kind = 4; src = w2; ld = 448; n0 = ((b - 168) % 14) * 32; kc = ch + ((b - 168) / 14) * 56; }
  const int n = n0 + cq * 4;
  const bool act = (kind == 3) || ch < 56;
  float cs[4] = {0.f, 0.f, 0.f, 0.f};
  unsigned csoff = 0, rstep2 = 2;
  if (act) {
    u32x2 v[8];
    const int q = kc & 3, ks = kc >> 2;
#pragma unroll
    for (int j = 0; j < 8; ++j) {
      int row = kc * 8 + j; bool ok = true;
      if (kind == 3) { int f = 16 * (j >> 2) + 4 * q + (j & 3); ok = f < 28; row = ks * 28 + (ok ? f : 0); }
      v[j] = *(const u32x2*)(src + (size_t)row * ld + n);
      if (!ok) v[j] = u32x2{0, 0};
    }
    unsigned dst; int r0; bool padz = false;
    if (kind == 0) { int part = n / 448, nn = n % 448, hd = nn / 28, f = nn % 28, nt = 2 * part + (f >> 4);
      r0 = f & 15; dst = O_WQ + ((hd * 14 + ks) * 6 + nt) * 1024; csoff = O_CSQ + (hd * 96 + nt * 16) * 4; padz = (f == 24); }
    else if (kind <= 2) { int c = n / 112, nt = (n % 112) >> 3; r0 = ((n & 4) ? 8 : 0) + (kind == 2 ? 2 : 0); rstep2 = 4;
      dst = O_W13 + ((c * 14 + ks) * 14 + nt) * 1024; csoff = O_CS13 + (c * 224 + nt * 16) * 4; }
    else { int cg = n / 112, nt = (n % 112) >> 4; r0 = n & 15;
      dst = (kind == 3) ? O_WO + ((cg * 16 + ks) * 7 + nt) * 1024 : O_W2 + ((cg * 56 + ks) * 7 + nt) * 1024; }
    dst += q * 256; csoff += r0 * 4;
#pragma unroll
    for (int i = 0; i < 4; ++i) {
      u32x4 g;
#pragma unroll
      for (int j = 0; j < 4; ++j) {
        unsigned a = v[2 * j][i >> 1], c = v[2 * j + 1][i >> 1];
        g[j] = (i & 1) ? __builtin_amdgcn_perm(c, a, 0x07060302u) : __builtin_amdgcn_perm(c, a, 0x05040100u);
        cs[i] += blo(g[j]) + bhi(g[j]);
      }
      *(u32x4*)(ws + dst + (r0 + (i & 1) + (i >> 1) * rstep2) * 16) = g;
      if (padz) *(u32x4*)(ws + dst + (12 + i) * 16) = u32x4{0, 0, 0, 0};
    }
    if (padz && ch == 0) *(f32x4*)(ws + csoff + 16) = f32x4{0.f, 0.f, 0.f, 0.f};
  }
  if (kind <= 2) {
    if (act) { for (int i = 0; i < 4; ++i) red[ch][cq * 4 + i] = cs[i]; }
    __syncthreads();
    if (ch < 4) {
      float s = 0.f;
      for (int k = 0; k < 56; ++k) s += red[k][cq * 4 + ch];
      *(float*)(ws + csoff + ((ch & 1) + (ch >> 1) * rstep2) * 4) = s;
    }
  }
}

struct Ctx { rsrc_t ws; gu32* cnt; char* sm; int w, l, g, c; unsigned v16;
  DEV void fresh() { asm volatile("" : "+v"(l)); v16 = l * 16; } };
#define SB() __builtin_amdgcn_sched_barrier(0)
#define TS(i)
#define TX(i)

DEV void publish(const Ctx& C, int idx) {
  wait_vm<0>(); bar();
  if (threadIdx.x == 0) __hip_atomic_fetch_add(C.cnt + (idx * 16 + C.g) * 64, 1u, __ATOMIC_RELAXED, __HIP_MEMORY_SCOPE_AGENT);
}
DEV void poll(gu32* p) {
#pragma unroll 1
  for (unsigned s = 0; s < 400000u; ++s) {
    if ((unsigned)__builtin_amdgcn_readfirstlane(__hip_atomic_load(p, __ATOMIC_RELAXED, __HIP_MEMORY_SCOPE_AGENT)) >= 16u) break;
    __builtin_amdgcn_s_sleep(1);
  }
}
DEV void await(const Ctx& C, int idx, bool halo) {
  if (C.w == 0) { poll(C.cnt + (idx * 16 + C.g) * 64); if (halo && (C.g & 7)) poll(C.cnt + (idx * 16 + C.g - 1) * 64); }
  bar();
}
template <bool GLU> DEV void stats_to_lds(const Ctx& C, u32x4 a, u32x4 b, int slot) {
  float m0 = __uint_as_float(a[0]), m1 = __uint_as_float(a[2]), m2 = __uint_as_float(b[0]), m3 = __uint_as_float(b[2]);
  float mean = 0.25f * ((m0 + m1) + (m2 + m3));
  m0 -= mean; m1 -= mean; m2 -= mean; m3 -= mean;
  float M2 = (__uint_as_float(a[1]) + __uint_as_float(a[3])) + (__uint_as_float(b[1]) + __uint_as_float(b[3])) + 112.f * ((m0 * m0 + m1 * m1) + (m2 * m2 + m3 * m3));
  const float ve = M2 * (1.f / 448.f) + 1e-5f, rstd = __builtin_amdgcn_rsqf(ve);
  if constexpr (GLU) *(f32x4*)(C.sm + L_ST + slot * 16) = f32x4{-mean, rstd * -1.4426950408889634f, ve, 0.f};
  else *(f32x2*)(C.sm + L_ST + slot * 8) = f32x2{mean, rstd};
}
template <int B, int E> DEV void wq_dma(const Ctx& C) {
  const unsigned gb = opq(O_WQ + (C.c * 84 + C.w * 21) * 1024), lb = opq(C.w * 21 * 1024);
  sfor<B, E>([&](auto I) { dma16<false>(C.ws, C.v16, gb + I.v * 1024, lb + I.v * 1024); });
}
template <class PF> DEV void attend(const Ctx& C, u32x4 Q, u32x4 Kp, u32x4 Vp, u32x4 Ko, u32x4 Vo, bool hasprev, unsigned dst, PF&& pf) {
  const f32x4 z = {0.f, 0.f, 0.f, 0.f};
  f32x4 s0 = mfma(Kp, Q, z), s1 = mfma(Ko, Q, z);
  const int d = (C.l & 15) - 4 * (C.l >> 4);
  float m = -3e38f;
#pragma unroll
  for (int i = 0; i < 4; ++i) {
    s0[i] = (hasprev && d - i + 16 <= 7) ? s0[i] : -3e38f;
    s1[i] = ((unsigned)(d - i) <= 7u) ? s1[i] : -3e38f;
    m = fmaxf(m, fmaxf(s0[i], s1[i]));
  }
  pf(IC<0>{}); m = rows_max(m);
  const float sc = 0.18898223650461362f * 1.4426950408889634f;
  float sum = 0.f;
#pragma unroll
  for (int i = 0; i < 4; ++i) {
    s0[i] = __builtin_amdgcn_exp2f((s0[i] - m) * sc); s1[i] = __builtin_amdgcn_exp2f((s1[i] - m) * sc);
    sum += s0[i] + s1[i];
  }
  pf(IC<1>{});
  const float r = __builtin_amdgcn_rcpf(rows_sum(sum));
  u32x4 P = {pk(s0[0] * r, s0[1] * r), pk(s0[2] * r, s0[3] * r), pk(s1[0] * r, s1[1] * r), pk(s1[2] * r, s1[3] * r)};
  f32x4 o0 = mfma(u32x4{Vp[0], Vp[1], Vo[0], Vo[1]}, P, z), o1 = mfma(u32x4{Vp[2], Vp[3], Vo[2], Vo[3]}, P, z);
  st16<16>(u32x4{pk(o0[0], o0[1]), pk(o0[2], o0[3]), pk(o1[0], o1[1]), pk(o1[2], o1[3])}, C.ws, C.v16, dst);
}
DEV u32x2 normT(f32x4 a, f32x4 cs, f32x2 st) {
  return u32x2{pk(st[1] * (a[0] - st[0] * cs[0]), st[1] * (a[1] - st[0] * cs[1])), pk(st[1] * (a[2] - st[0] * cs[2]), st[1] * (a[3] - st[0] * cs[3]))};
}
DEV u32x2 normF(f32x4 a, float cs, f32x4 s01, f32x4 s23) {
  return u32x2{pk(s01[1] * (a[0] - s01[0] * cs), s01[3] * (a[1] - s01[2] * cs)), pk(s23[1] * (a[2] - s23[0] * cs), s23[3] * (a[3] - s23[2] * cs))};
}
template <bool FINAL, class PF> DEV void ksplit_epilogue(const Ctx& C, f32x4 (&acc)[2][7], u32x4 (&res)[2], unsigned xdst, unsigned stdst, rsrc_t out, PF&& pf) {
  const int q = C.l >> 4, tok = C.l & 15;
  sfor<0, 14>([&](auto I) { constexpr int m = I.v / 7, nt = I.v % 7;
    *(f32x4*)(C.sm + L_RED + (C.w * 14 + I.v) * 1024 + q * 256 + ((tok + 8 * (nt & 1)) & 15) * 16) = acc[m][nt];
    if constexpr (I.v % 3 == 1) pf(IC<I.v / 3>{}); });
  bar();
  const int g8 = C.l >> 3, m = C.w >> 1, tk = 8 * (C.w & 1) + (C.l & 7), cg = ROLE_CG(C.c), mt = C.g * 8 + ROLE_MP(C.c) * 2 + m;
  float val[2][8]; float s = 0.f;
  sfor<0, 2>([&](auto H) { constexpr int h = H.v;
    const int gr = min(g8 + 8 * h, 13), nt = gr >> 1;
    const char* p = C.sm + L_RED + (m * 7 + nt) * 1024 + (gr & 1) * 512 + ((tk + 8 * (nt & 1)) & 15) * 16;
    f32x4 a = *(const f32x4*)p, b = *(const f32x4*)(p + 256);
#pragma unroll
    for (int k = 1; k < 4; ++k) { a += *(const f32x4*)(p + k * 14336); b += *(const f32x4*)(p + k * 14336 + 256); }
    pf(IC<5 + 2 * h>{});
    u32x4 r = res[h];
    r = u32x4{pk(a[0] + blo(r[0]), a[1] + bhi(r[0])), pk(a[2] + blo(r[1]), a[3] + bhi(r[1])), pk(b[0] + blo(r[2]), b[1] + bhi(r[2])), pk(b[2] + blo(r[3]), b[3] + bhi(r[3]))};
    res[h] = r;
    pf(IC<6 + 2 * h>{});
    const bool ok = h == 0 || g8 < 6;
    const int G = cg * 14 + gr;
    if constexpr (FINAL) { const int u = mt * 16 + tk; if (ok) st16<0>(r, out, (2 * (u & 1023) + (u >> 10)) * 896 + G * 16, 0); }
    else {
      if (ok) st16<16>(r, C.ws, (mt * 14 + (G >> 2)) * 1024 + (G & 3) * 256 + tk * 16, xdst);
#pragma unroll
      for (int j = 0; j < 4; ++j) { val[h][2 * j] = blo(r[j]); val[h][2 * j + 1] = bhi(r[j]); s += ok ? val[h][2 * j] + val[h][2 * j + 1] : 0.f; }
    }
  });
  pf(IC<9>{});
  if constexpr (!FINAL) {
    const float mean = sum8(s) * (1.f / 112.f); float m2 = 0.f, m2b = 0.f;
#pragma unroll
    for (int j = 0; j < 8; ++j) { float a = val[0][j] - mean, b = val[1][j] - mean; m2 += a * a; m2b += b * b; }
    m2 = sum8(g8 < 6 ? m2 + m2b : m2);
    asm volatile("" : "+v"(res[0]), "+v"(res[1]));
    if (g8 == 0) __builtin_amdgcn_raw_buffer_store_b64(u32x2{__float_as_uint(mean), __float_as_uint(m2)}, C.ws, (mt * 16 + tk) * 32 + cg * 8, stdst, 16);
  }
}

__global__ __launch_bounds__(256, 1) void main_kernel(char* wsp, uint16_t* outp) {
  __shared__ __attribute__((aligned(1024))) char sm[L_END + 256];
  Ctx C;
  C.ws = __builtin_amdgcn_make_buffer_rsrc(wsp, 0, WS_END, 0x00020000);
  const rsrc_t out = __builtin_amdgcn_make_buffer_rsrc(outp, 0, 2048 * 448 * 2, 0x00020000);
  C.cnt = (gu32*)wsp; C.sm = sm;
  C.w = __builtin_amdgcn_readfirstlane(threadIdx.x >> 6); C.l = threadIdx.x & 63; C.v16 = C.l * 16;
  { const int b = blockIdx.x, x = b & 7, s = b >> 3;
    if (XMAP == 0) { C.g = 2 * x + (s >> 4); C.c = s & 15; } else { C.g = 4 * (x >> 1) + (s >> 3); C.c = (x & 1) * 8 + (s & 7); } }
  const int w = C.w, g = C.g, c = C.c, cg = ROLE_CG(c);
  const f32x4 z4 = {0.f, 0.f, 0.f, 0.f};
  const unsigned mp0 = g * 8 + ROLE_MP(c) * 2;
  wq_dma<0, 21>(C);
  u32x4 res[2];
#pragma unroll
  for (int h = 0; h < 2; ++h) { const int l = C.l, G = cg * 14 + min((l >> 3) + 8 * h, 13);
    res[h] = ld16<0>(C.ws, ((mp0 + (w >> 1)) * 14 + (G >> 2)) * 1024 + (G & 3) * 256 + (8 * (w & 1) + (l & 7)) * 16, O_X); }

#pragma unroll 1
  for (int L = 0; L < 2; ++L) {
    u32x4 wo[28], w13[56], w2[98];
    {
      C.fresh(); const int q = C.l >> 4, tok = C.l & 15, t = w * 64 + C.l;
      const unsigned Xin = O_X + 2 * L * X_SZ, Sin = O_ST + 2 * L * ST_SZ;
      const f32x4 csq0 = __builtin_bit_cast(f32x4, ld16<0>(C.ws, q * 16, O_CSQ + c * 384)), csq1 = __builtin_bit_cast(f32x4, ld16<0>(C.ws, q * 16, O_CSQ + c * 384 + 64));
      const f32x4 csk0 = __builtin_bit_cast(f32x4, ld16<0>(C.ws, q * 16, O_CSQ + c * 384 + 128)), csk1 = __builtin_bit_cast(f32x4, ld16<0>(C.ws, q * 16, O_CSQ + c * 384 + 192));
      const float csv0 = __uint_as_float(__builtin_amdgcn_raw_buffer_load_b32(C.ws, tok * 4, O_CSQ + c * 384 + 256, 0));
      const float csv1 = __uint_as_float(__builtin_amdgcn_raw_buffer_load_b32(C.ws, tok * 4, O_CSQ + c * 384 + 320, 0));
      TS(0); TX(0); wait_vm<0>();
      if (L) await(C, 3, true); else bar();
      TS(1); TX(1);
      const unsigned mh = (g & 7) ? 8 * g - 1 : 8 * g, ma = 8 * g + 2 * w;
      const int sl = min(t, 143); const unsigned su = g * 128 - 16 + sl + (sl < ((g & 7) ? 0 : 16) ? 16 : 0);
      u32x4 sa = ld16<16>(C.ws, su * 32, Sin), sb = ld16<16>(C.ws, su * 32 + 16, Sin);
      u32x4 xh[4], xa[14], xb[14];
      const unsigned hb = opq(Xin + mh * 14336 + w * 1024), h3 = opq(Xin + mh * 14336 + (12 + (w & 1)) * 1024), ab_ = opq(Xin + ma * 14336), wob = opq(O_WO + (cg * 16 + w * 4) * 7168);
      sfor<0, NX1>([&](auto I) { xa[I.v] = ldf<16, I.v>(C.ws, C.v16, ab_); xb[I.v] = ldf<16, 14 + I.v>(C.ws, C.v16, ab_); });
      SB();
      stats_to_lds<false>(C, sa, sb, sl);
      TX(2);
      const char* wl = sm + C.v16;
      f32x4 aa[6], ab[6], ah[4];
      sfor<0, 6>([&](auto N) { aa[N.v] = z4; ab[N.v] = z4; ah[N.v & 3] = z4; });
      constexpr int D1 = 5; u32x4 W[100];
      const char* wh = wl + w * 6144; const char* wh3 = wl + (12 + (w & 1)) * 6144;
      auto rdw = [&](auto I) { constexpr int j = I.v - 84;
        if constexpr (j < 0) W[I.v] = *(const u32x4*)(wl + I.v * 1024);
        else if constexpr (j < 12) W[I.v] = *(const u32x4*)(wh + (j / 4 * 24 + 2 + j % 4) * 1024);
        else if constexpr (j < 16) W[I.v] = *(const u32x4*)(wh3 + (j - 10) * 1024); };
      sfor<0, D1>(rdw);
      SB();
      sfor<0, 84>([&](auto I) { constexpr int k = I.v / 6, n = I.v % 6;
        rdw(IC<I.v + D1>{});
        if constexpr (n == 0 && k + NX1 < 14) xa[k + NX1] = ldf<16, k + NX1>(C.ws, C.v16, ab_);
        if constexpr (n == 2 && k + NX1 < 14) xb[k + NX1] = ldf<16, 14 + k + NX1>(C.ws, C.v16, ab_);
        if constexpr (n == 4 && k >= 4 && k < 8) xh[k - 4] = ld16<16>(C.ws, C.v16, k < 7 ? hb + (k - 4) * 4096 : h3);
        if constexpr (n < 4) { aa[n] = mfma(W[I.v], xa[k], aa[n]); ab[n] = mfma(W[I.v], xb[k], ab[n]); }
        else { aa[n] = mfma(xa[k], W[I.v], aa[n]); ab[n] = mfma(xb[k], W[I.v], ab[n]); }
        if constexpr (I.v == 5) { TX(3); } if constexpr (I.v == 41) { TX(4); }
        SB(); });
      if (w >= 2) xh[3] = u32x4{0, 0, 0, 0};
      sfor<0, 16>([&](auto J) { constexpr int n = J.v % 4;
        rdw(IC<84 + J.v + D1>{});
        if constexpr (n < 2) ah[n] = mfma(W[84 + J.v], xh[J.v / 4], ah[n]); else ah[n] = mfma(xh[J.v / 4], W[84 + J.v], ah[n]);
        SB(); });
      sfor<0, 4>([&](auto N) { *(f32x4*)(sm + L_RED + (w * 4 + N.v) * 1024 + C.v16) = ah[N.v]; });
      TS(2); TX(5); bar();
      const char* sp = sm + L_ST;
      auto pwo = [&](auto I) { SB(); wo[2 * I.v] = ldf<0, 2 * I.v>(C.ws, C.v16, wob); wo[2 * I.v + 1] = ldf<0, 2 * I.v + 1>(C.ws, C.v16, wob); SB(); };
      pwo(IC<0>{});
      { u32x2 hv; const char* hp = sm + L_RED + w * 1024 + C.v16;
        const f32x4 hs = (*(const f32x4*)hp + *(const f32x4*)(hp + 4096)) + (*(const f32x4*)(hp + 8192) + *(const f32x4*)(hp + 12288));
        if (w < 2) hv = normT(hs, w ? csk1 : csk0, *(const f32x2*)(sp + tok * 8));
        else hv = normF(hs, w == 2 ? csv0 : csv1, *(const f32x4*)(sp + q * 32), *(const f32x4*)(sp + q * 32 + 16));
        *(u32x2*)(sm + L_EX + (w >> 1) * 1024 + C.v16 + (w & 1) * 8) = hv; }
      auto cat = [](u32x2 a, u32x2 b) { return u32x4{a[0], a[1], b[0], b[1]}; };
      const char* spb = sp + (32 + 32 * w) * 8; const char* spa = spb - 128;
      const f32x2 tb = *(const f32x2*)(spb + tok * 8), ta = *(const f32x2*)(spa + tok * 8);
      const f32x4 b01 = *(const f32x4*)(spb + q * 32), b23 = *(const f32x4*)(spb + q * 32 + 16), a01 = *(const f32x4*)(spa + q * 32), a23 = *(const f32x4*)(spa + q * 32 + 16);
      auto nT = [&](f32x4 a0, f32x4 a1, f32x4 c0, f32x4 c1, f32x2 st, auto I) { pwo(I); u32x2 x = normT(a0, c0, st); pwo(IC<I.v + 1>{}); return cat(x, normT(a1, c1, st)); };
      auto nF = [&](f32x4 a0, f32x4 a1, f32x4 s01, f32x4 s23, auto I) { pwo(I); u32x2 x = normF(a0, csv0, s01, s23); pwo(IC<I.v + 1>{}); return cat(x, normF(a1, csv1, s01, s23)); };
      const u32x4 Kb = nT(ab[2], ab[3], csk0, csk1, tb, IC<1>{}), Vb = nF(ab[4], ab[5], b01, b23, IC<3>{});
      *(u32x4*)(sm + L_EX + (w + 1) * 2048 + C.v16) = Kb; *(u32x4*)(sm + L_EX + (w + 1) * 2048 + 1024 + C.v16) = Vb;
      const u32x4 Ka = nT(aa[2], aa[3], csk0, csk1, ta, IC<5>{}), Va = nF(aa[4], aa[5], a01, a23, IC<7>{});
      const u32x4 Qb = nT(ab[0], ab[1], csq0, csq1, tb, IC<9>{});
      const unsigned od = O_O + L * O_SZ + (ma * 16 + c) * 1024;
      attend(C, Qb, Ka, Va, Kb, Vb, true, od + 16384, [&](auto I) { pwo(IC<11 + I.v>{}); });
      pwo(IC<13>{});
      const u32x4 Qa = cat(normT(aa[0], csq0, ta), normT(aa[1], csq1, ta));
      bar();
      const u32x4 Kp = *(const u32x4*)(sm + L_EX + w * 2048 + C.v16), Vp = *(const u32x4*)(sm + L_EX + w * 2048 + 1024 + C.v16);
      attend(C, Qa, Kp, Vp, Ka, Va, w > 0 || (g & 7), od, [](auto) {});
      TS(3); publish(C, L * 4 + 0); TS(4);
    }
    const unsigned b13 = opq(O_W13 + (c * 196 + 3 * w) * 1024), b13x = opq(O_W13 + (c * 196 + 12 + (w >> 1)) * 1024), b2 = opq(O_W2 + (cg * 56 + w * 14) * 7168);
    auto ld13 = [&](auto I) { constexpr int ks = I.v / 4, j = I.v % 4;
      w13[I.v] = ld16<0>(C.ws, C.v16 + (j < 3 ? j * 1024 : 0), (j < 3 ? b13 : b13x) + ks * 14336); };
    auto ld2 = [&](auto I) { if constexpr (I.v < 63) w2[35 + I.v] = ldf<0, 35 + I.v>(C.ws, C.v16, b2); };
    const unsigned la = opq(w * 21504), lb = opq(L_RED + w * 14336 - 21504);
    auto dm2 = [&](auto I) { if constexpr (I.v < 35) dma16<false>(C.ws, C.v16, b2 + I.v * 1024, (I.v < 21 ? la : lb) + I.v * 1024); };
    {
      C.fresh();
      sfor<0, N13A>(ld13);
      await(C, L * 4 + 0, false); TS(5);
      u32x4 of[2][4];
      const unsigned ob = opq(O_O + L * O_SZ + (mp0 * 16 + w * 4) * 1024);
      sfor<0, 8>([&](auto I) { of[I.v & 1][I.v >> 1] = ldf<16, (I.v & 1) * 16 + (I.v >> 1)>(C.ws, C.v16, ob); });
      __builtin_amdgcn_sched_barrier(0);
      f32x4 acc[2][7];
      sfor<0, 14>([&](auto I) { acc[I.v / 7][I.v % 7] = z4; });
      sfor<0, 4>([&](auto K) { sfor<0, 7>([&](auto N) { acc[0][N.v] = mfma(wo[K.v * 7 + N.v], of[0][K.v], acc[0][N.v]); acc[1][N.v] = mfma(wo[K.v * 7 + N.v], of[1][K.v], acc[1][N.v]); }); });
      C.fresh();
      ksplit_epilogue<false>(C, acc, res, O_X + (2 * L + 1) * X_SZ, O_ST + (2 * L + 1) * ST_SZ, out,
                             [&](auto I) { SB(); sfor<N13A + N13B * I.v, N13A + N13B * (I.v + 1)>(ld13); SB(); });
      TS(6); publish(C, L * 4 + 1); TS(7);
    }
    {
      C.fresh();
      sfor<N13A + 10 * N13B, 56>(ld13);
      await(C, L * 4 + 1, false); TS(8); TX(6);
      const unsigned Xin = O_X + (2 * L + 1) * X_SZ, Sin = O_ST + (2 * L + 1) * ST_SZ;
      const int sl = (w & 1) * 64 + C.l; const unsigned su = g * 128 + sl;
      u32x4 sa = ld16<16>(C.ws, su * 32, Sin), sb = ld16<16>(C.ws, su * 32 + 16, Sin);
      const unsigned xg = opq(Xin + (g * 8 + w) * 14336), xs = opq(w * 1024);
      auto dmx = [&](auto I) { constexpr int h = I.v / 14, ks = I.v % 14; if constexpr (I.v < 28) dma16<true>(C.ws, C.v16, xg + h * 57344 + ks * 1024, xs + I.v * 4096); };
      sfor<0, 6>(dmx);
      f32x4 acc[8][3], ax[4], cs[4], st[8];
      sfor<0, 8>([&](auto M) { acc[M.v][0] = z4; acc[M.v][1] = z4; acc[M.v][2] = z4; ax[M.v & 3] = z4; });
      const int q = C.l >> 4, tok = C.l & 15, mx = 2 * (w & 1);
      const char* x0 = sm + C.v16 + mx * 1024; const char* x1 = sm + C.v16 + (mx ^ 2) * 1024 - 2048;
      constexpr int D3 = 4; u32x4 X[112];
      auto rdx = [&](auto I) { if constexpr (I.v < 112) X[I.v] = *(const u32x4*)(((I.v & 2) ? x1 : x0) + I.v * 1024); };
      auto rst = [&](auto M) { st[M.v] = *(const f32x4*)(sm + L_ST + ((M.v ^ mx) * 16 + tok) * 16); };
      const unsigned Hd = opq(O_H + L * H_SZ + g * 8 * 57344), hv0 = (q ^ mx) * 57344 + tok * 16;
      f32x2 ga, ua, tg, tu, ar, pr; unsigned hv[4]; u32x2 ac, bd, y01, y23;
      auto item = [&](auto S, const f32x4& a, const f32x4& cn, const f32x4& sv, unsigned& h) { constexpr int t = S.v;
        if constexpr (t == 0) { ga = f32x2{a[0], a[1]}; asm volatile("" : "+v"(ga)); }
        if constexpr (t == 1) { ua = f32x2{a[2], a[3]}; asm volatile("" : "+v"(ua)); }
        if constexpr (t == 2) tg = ga + sv[0] * f32x2{cn[0], cn[1]};
        if constexpr (t == 3) tu = ua + sv[0] * f32x2{cn[2], cn[3]};
        if constexpr (t == 4) ar = tg * sv[1];
        if constexpr (t == 5) ar[0] = __builtin_amdgcn_exp2f(ar[0]);
        if constexpr (t == 6) ar[1] = __builtin_amdgcn_exp2f(ar[1]);
        if constexpr (t == 7) pr = tg * tu;
        if constexpr (t == 8) ar = ar * sv[2] + sv[2];
        if constexpr (t == 9) ar[0] = __builtin_amdgcn_rcpf(ar[0]);
        if constexpr (t == 10) ar[1] = __builtin_amdgcn_rcpf(ar[1]);
        if constexpr (t == 12) pr = pr * ar;
        if constexpr (t == 13) h = pk(pr[0], pr[1]); };
      auto xpose = [&](auto S, int nt, unsigned voff, unsigned soff) { constexpr int t = S.v;
        if constexpr (t == 2) ac = __builtin_amdgcn_permlane32_swap(hv[0], hv[2], false, false);
        if constexpr (t == 3) bd = __builtin_amdgcn_permlane32_swap(hv[1], hv[3], false, false);
        if constexpr (t == 7) y01 = __builtin_amdgcn_permlane16_swap(ac[0], bd[0], false, false);
        if constexpr (t == 8) y23 = __builtin_amdgcn_permlane16_swap(ac[1], bd[1], false, false);
        if constexpr (t == 12) { const unsigned G = 14 * c + nt; st16<16>(u32x4{y01[0], y01[1], y23[0], y23[1]}, C.ws, voff, soff + (G >> 2) * 1024 + (G & 3) * 256); } };
      wait_vm<4>(); stats_to_lds<true>(C, sa, sb, sl); bar(); TX(7);
      sfor<0, D3>(rdx);
      SB();
      sfor<0, 112>([&](auto I) { constexpr int h = I.v / 56, k = I.v / 4 % 14, m = I.v % 4, t0 = m < 2 ? 4 * m : 2 + 3 * m;
        if constexpr (I.v == 4) { wait_vm<2>(); bar(); TX(8); }
        if constexpr (I.v == 20) { wait_vm<6>(); bar(); }
        if constexpr (I.v == 36) { wait_vm<10>(); bar(); TX(9); }
        if constexpr (I.v == 52) { wait_vm<10 + 2 * N2A>(); bar(); }
        if constexpr (I.v == 84) { wait_vm<4 + 3 * N2A + 6 * PF3>(); bar(); TX(10); }
        rdx(IC<I.v + D3>{});
        sfor<0, (m < 2 ? 4 : 3)>([&](auto J) { constexpr int t = t0 + J.v;
          if constexpr (J.v < 3) acc[4 * h + m][J.v] = mfma(w13[k * 4 + J.v], X[I.v], acc[4 * h + m][J.v]);
          else ax[2 * h + m] = mfma(w13[k * 4 + 3], X[I.v], ax[2 * h + m]);
          if constexpr (h == 0) {
            if constexpr (t == 1 || t == 8) dmx(IC<6 + 2 * k + t / 8>{});
            if constexpr (k >= 11 && t % 4 == 2 && t / 4 < N2A) ld2(IC<(k - 11) * N2A + t / 4>{});
            if constexpr (k == 11 && t % 4 == 0) cs[t / 4] = __builtin_bit_cast(f32x4, ld16<0>(C.ws, q * 16, O_CS13 + (c * 224 + (t < 12 ? 3 * w + t / 4 : 12 + (w >> 1)) * 16) * 4));
            if constexpr (k == 13 && t % 4 == 0) rst(IC<t / 4>{});
          } else {
            if constexpr (k > 0 && t % 3 == 1 && t / 3 < PF3) ld2(IC<3 * N2A + (k - 1) * PF3 + t / 3>{});
            if constexpr (k >= 1 && k <= 12) item(IC<t>{}, acc[(k - 1) % 4][(k - 1) / 4], cs[(k - 1) / 4], st[(k - 1) % 4], hv[(k - 1) % 4]);
            if constexpr (k == 5 || k == 9 || k == 13) xpose(IC<t>{}, 3 * w + (k - 5) / 4, hv0, Hd);
            if constexpr (k == 13 && t % 4 == 0) rst(IC<4 + t / 4>{});
          }
          SB(); }); });
      bar();
      TS(9); TX(11);
      sfor<0, 4>([&](auto U) { constexpr int j = U.v;
        sfor<ND2 * j, ND2 * (j + 1)>(dm2); SB();
        sfor<0, 4>([&](auto I) { constexpr int i = I.v;
          sfor<0, 14>([&](auto S) { if constexpr (j < 3) item(S, acc[4 + i][j], cs[j], st[4 + i], hv[i]); else item(S, ax[i], cs[3], st[(i & 1) + 4 * (i >> 1)], hv[i]); }); });
        sfor<0, 14>([&](auto S) { if constexpr (j < 3) xpose(S, 3 * w + j, hv0, Hd + 4 * 57344);
          else xpose(S, 12 + (w >> 1), ((q & 1) + mx + 4 * (q >> 1)) * 57344 + tok * 16, Hd); });
        SB(); });
      TS(10); publish(C, L * 4 + 2); TS(11);
    }
    {
      C.fresh();
      sfor<4 * ND2, 35>(dm2); sfor<3 * N2A + 13 * PF3, 63>(ld2);
      await(C, L * 4 + 2, false); TS(12); TX(12);
      const unsigned Hs = opq(O_H + L * H_SZ + (mp0 * 56 + w * 14) * 1024);
      u32x4 hf[2][14];
      auto ldh = [&](auto K) { hf[0][K.v] = ldf<16, K.v>(C.ws, C.v16, Hs); hf[1][K.v] = ldf<16, 56 + K.v>(C.ws, C.v16, Hs); };
      sfor<0, NH0>(ldh);
      wait_vm<2 * NH0>();
      const char* pa = sm + w * 21504 + C.v16; const char* pb = sm + L_RED + w * 14336 - 21504 + C.v16;
      constexpr int D4 = 4, KQ = 11;
      auto rd2 = [&](auto I) { if constexpr (I.v < 35) w2[I.v] = *(const u32x4*)((I.v < 21 ? pa : pb) + I.v * 1024); };
      sfor<0, D4>(rd2);
      SB();
      f32x4 acc[2][7];
      sfor<0, 14>([&](auto I) { acc[I.v / 7][I.v % 7] = z4; });
      sfor<0, 14>([&](auto K) {
        if constexpr (K.v == KQ) asm volatile("" :: "v"(hf[0][13]), "v"(hf[1][13]));
        if constexpr (K.v >= KQ) { if (L == 0) wq_dma<3 * (K.v - KQ), 3 * (K.v - KQ + 1)>(C); }
        sfor<0, 7>([&](auto N) { rd2(IC<K.v * 7 + N.v + D4>{});
          acc[0][N.v] = mfma(w2[K.v * 7 + N.v], hf[0][K.v], acc[0][N.v]); acc[1][N.v] = mfma(w2[K.v * 7 + N.v], hf[1][K.v], acc[1][N.v]);
          if constexpr (K.v + NH0 < 14 && N.v == 1) hf[0][K.v + NH0] = ldf<16, K.v + NH0>(C.ws, C.v16, Hs);
          if constexpr (K.v + NH0 < 14 && N.v == 4) hf[1][K.v + NH0] = ldf<16, 56 + K.v + NH0>(C.ws, C.v16, Hs);
          SB(); });
        if constexpr (K.v == 0) { TX(13); } if constexpr (K.v == 6) { TX(14); }
      });
      TS(13); TX(15); C.fresh();
      if (L == 0) { ksplit_epilogue<false>(C, acc, res, O_X + 2 * X_SZ, O_ST + 2 * ST_SZ, out, [&](auto I) { constexpr int b = 3 * (14 - KQ) + 2 * I.v; SB(); wq_dma<(b < 21 ? b : 21), (b + 2 < 21 ? b + 2 : 21)>(C); SB(); }); TS(14); publish(C, 3); TS(15); }
      else { ksplit_epilogue<true>(C, acc, res, 0, 0, out, [](auto) {}); TS(14); TS(15); }
    }
  }
}

extern "C" void kernel_launch(void* const* d_in, const int* in_sizes, int n_in, void* d_out, int out_size, void* d_ws, size_t ws_size,
                              hipStream_t stream) {
  prep_kernel<<<240, 512, 0, stream>>>((const uint16_t*)d_in[0], (const uint16_t*)d_in[1], (const uint16_t*)d_in[2], (const uint16_t*)d_in[4],
                                       (const uint16_t*)d_in[5], (const uint16_t*)d_in[6], (char*)d_ws);
  main_kernel<<<256, 256, 0, stream>>>((char*)d_ws, (uint16_t*)d_out);
}
```

```cpp
#include <hip/hip_runtime.h>
#include <hip/hip_bf16.h>
#include <stdint.h>

typedef __attribute__((ext_vector_type(8))) __bf16 bf16x8;
typedef __attribute__((ext_vector_type(2))) __bf16 bf16x2;
typedef __attribute__((ext_vector_type(4))) float f32x4;
typedef __attribute__((ext_vector_type(2))) float f32x2;
typedef __attribute__((ext_vector_type(4))) unsigned u32x4;
typedef __attribute__((ext_vector_type(2))) unsigned u32x2;
typedef __attribute__((address_space(1))) unsigned gu32;
typedef __amdgpu_buffer_rsrc_t rsrc_t;

#define XMAP 1
#define CMAP 1
#define ROLE_CG(c) (CMAP ? (c) & 3 : (c) >> 2)
#define ROLE_MP(c) (CMAP ? (c) >> 2 : (c) & 3)
#define N13A 10
#define N13B 2
#define N13C 0
#define NWO 6
#define ND4 0
#define POLLP 1
#define NX1 4
#define N2A 2
#define PF3 4
#define ND2 4
#define ND3 10
#define NH0 6

#define CPF 1
constexpr unsigned O_CNT = 0;
constexpr unsigned O_CSQ = 32768;
constexpr unsigned O_CS13 = O_CSQ + 6144;
constexpr unsigned O_ST = 65536;
constexpr unsigned ST_SZ = 65536;
constexpr unsigned O_WQ = O_ST + 4 * ST_SZ;
constexpr unsigned O_WO = O_WQ + 16 * 14 * 6 * 1024;
constexpr unsigned O_W13 = O_WO + 4 * 16 * 7 * 1024;
constexpr unsigned O_W2 = O_W13 + 16 * 14 * 14 * 1024;
constexpr unsigned O_X = O_W2 + 4 * 56 * 7 * 1024;
constexpr unsigned X_SZ = 128 * 14 * 1024;
constexpr unsigned O_O = O_X + 4 * X_SZ;
constexpr unsigned O_SZ = 128 * 16 * 1024;
constexpr unsigned O_H = O_O + 2 * O_SZ;
constexpr unsigned H_SZ = 128 * 56 * 1024;
constexpr unsigned WS_END = O_H + 2 * H_SZ;
constexpr unsigned L_RED = 86016, L_ST = 143360, L_EX = 144640, L_END = L_EX + 10240;

#define DEV __device__ __forceinline__
template <int N> struct IC { static constexpr int v = N; };
template <int B, int E, class F> DEV void sfor(F&& f) { if constexpr (B < E) { f(IC<B>{}); sfor<B + 1, E>(f); } }

DEV f32x4 mfma(u32x4 a, u32x4 b, f32x4 c) {
  return __builtin_amdgcn_mfma_f32_16x16x32_bf16(__builtin_bit_cast(bf16x8, a), __builtin_bit_cast(bf16x8, b), c, 0, 0, 0);
}
DEV unsigned pk(float lo, float hi) { f32x2 v = {lo, hi}; return __builtin_bit_cast(unsigned, __builtin_convertvector(v, bf16x2)); }
DEV float blo(unsigned u) { return __uint_as_float(u << 16); }
DEV float bhi(unsigned u) { return __uint_as_float(u & 0xffff0000u); }
template <int AUX> DEV u32x4 ld16(rsrc_t r, unsigned voff, unsigned soff) { return __builtin_amdgcn_raw_buffer_load_b128(r, voff, soff, AUX); }
template <int AUX> DEV void st16(u32x4 v, rsrc_t r, unsigned voff, unsigned soff) { __builtin_amdgcn_raw_buffer_store_b128(v, r, voff, soff, AUX); }
DEV unsigned opq(unsigned x) { x = __builtin_amdgcn_readfirstlane(x); asm volatile("" : "+s"(x)); return x; }
template <int AUX, int F> DEV u32x4 ldf(rsrc_t r, unsigned v16, unsigned base) { return ld16<AUX>(r, v16 + (F & 3) * 1024, base + (F >> 2) * 4096); }
template <bool SC1, int F> DEV void dma16(rsrc_t r, unsigned voff, unsigned soff, unsigned lds) {
  soff += (F >> 2) * 4096; lds += (F >> 2) * 4096;
  if constexpr (SC1) asm volatile("s_mov_b32 m0, %0\n\ts_nop 0\n\tbuffer_load_dwordx4 %1, %2, %3 offen offset:%4 sc1 lds" :: "s"(lds), "v"(voff), "s"(r), "s"(soff), "i"((F & 3) * 1024) : "memory");
  else asm volatile("s_mov_b32 m0, %0\n\ts_nop 0\n\tbuffer_load_dwordx4 %1, %2, %3 offen offset:%4 lds" :: "s"(lds), "v"(voff), "s"(r), "s"(soff), "i"((F & 3) * 1024) : "memory");
}
template <int N> DEV void wait_vm() { asm volatile("s_waitcnt vmcnt(%0)" :: "i"(N > 63 ? 63 : N) : "memory"); }
DEV void bar() { asm volatile("s_waitcnt lgkmcnt(0)\n\ts_barrier" ::: "memory"); }
DEV float x32(float x, float& y) { u32x2 r = __builtin_amdgcn_permlane32_swap(__float_as_uint(x), __float_as_uint(x), false, false); y = __uint_as_float(r[1]); return __uint_as_float(r[0]); }
DEV float x16(float x, float& y) { u32x2 r = __builtin_amdgcn_permlane16_swap(__float_as_uint(x), __float_as_uint(x), false, false); y = __uint_as_float(r[1]); return __uint_as_float(r[0]); }
DEV float a_rd(float a) { float r; asm volatile("v_accvgpr_read_b32 %0, %1" : "=v"(r) : "a"(a)); return r; }
DEV float a_fma(float a, float b, float c) { float r; asm volatile("v_fma_f32 %0, %1, %2, %3" : "=v"(r) : "v"(a), "v"(b), "v"(c)); return r; }
DEV float a_mul(float a, float b) { float r; asm volatile("v_mul_f32 %0, %1, %2" : "=v"(r) : "v"(a), "v"(b)); return r; }
DEV float a_exp(float a) { float r; asm volatile("v_exp_f32 %0, %1" : "=v"(r) : "v"(a)); return r; }
DEV float a_rcp(float a) { float r; asm volatile("v_rcp_f32 %0, %1" : "=v"(r) : "v"(a)); return r; }
DEV void pin_a(const f32x4& x) { asm volatile("" :: "a"(x)); }
DEV float rows_sum(float x) { float y; x = x32(x, y); x += y; x = x16(x, y); return x + y; }
DEV float sum8(float x) { x += __int_as_float(__builtin_amdgcn_update_dpp(0, __float_as_int(x), 0x128, 0xf, 0xf, false)); return rows_sum(x); }

__global__ __launch_bounds__(512) void prep_kernel(const uint16_t* __restrict__ x, const uint16_t* __restrict__ wqkv, const uint16_t* __restrict__ wo,
                                                   const uint16_t* __restrict__ w1, const uint16_t* __restrict__ w3, const uint16_t* __restrict__ w2,
                                                   char* __restrict__ ws) {
  __shared__ float red[56][33];
  const int b = (int)blockIdx.x - 64, t = threadIdx.x;
  if (blockIdx.x < 64) {
    const int wv = t >> 6, l = t & 63, mt = blockIdx.x * 2 + (wv >> 2), cg = wv & 3, q = l >> 4;
    if (t < 32) ((u32x4*)(ws + O_CNT))[blockIdx.x * 32 + t] = u32x4{0, 0, 0, 0};
    const int u = mt * 16 + (l & 15), tokn = 2 * (u & 1023) + (u >> 10);
    u32x4 v[4];
#pragma unroll
    for (int k = 0; k < 4; ++k) v[k] = *(const u32x4*)(x + (size_t)tokn * 448 + cg * 112 + min(q + 4 * k, 13) * 8);
    float s = 0.f, s3 = 0.f;
#pragma unroll
    for (int k = 0; k < 4; ++k) { const int G = cg * 14 + q + 4 * k;
      if (k < 3 || q < 2) *(u32x4*)(ws + O_X + (mt * 14 + (G >> 2)) * 1024 + (G & 3) * 256 + (l & 15) * 16) = v[k];
#pragma unroll
      for (int j = 0; j < 4; ++j) (k < 3 ? s : s3) += blo(v[k][j]) + bhi(v[k][j]);
    }
    const float mean = rows_sum(q < 2 ? s + s3 : s) * (1.f / 112.f);
    float m2 = 0.f, m3 = 0.f;
#pragma unroll
    for (int k = 0; k < 4; ++k)
#pragma unroll
      for (int j = 0; j < 4; ++j) { float a = blo(v[k][j]) - mean, c = bhi(v[k][j]) - mean; (k < 3 ? m2 : m3) += a * a + c * c; }
    m2 = rows_sum(q < 2 ? m2 + m3 : m2);
    if (q == 0) *(f32x2*)(ws + O_ST + u * 32 + cg * 8) = f32x2{mean, m2};
    return;
  }
  const int cq = t & 7, ch = t >> 3;
  const uint16_t* src; int ld, n0, kc = ch, kind;
  if (b < 42) { kind = 0; src = wqkv; ld = 1344; n0 = b * 32; }
  else if (b < 98) { kind = 1; src = w1; ld = 1792; n0 = (b - 42) * 32; }
  else if (b < 154) { kind = 2; src = w3; ld = 1792; n0 = (b - 98) * 32; }
  else if (b < 168) { kind = 3; src = wo; ld = 448; n0 = (b - 154) * 32; }
  else { kind = 4; src = w2; ld = 448; n0 = ((b - 168) % 14) * 32; kc = ch + ((b - 168) / 14) * 56; }
  const int n = n0 + cq * 4;
  const bool act = (kind == 3) || ch < 56;
  float cs[4] = {0.f, 0.f, 0.f, 0.f};
  unsigned csoff = 0, rstep2 = 2;
  if (act) {
    u32x2 v[8];
    const int q = kc & 3, ks = kc >> 2;
#pragma unroll
    for (int j = 0; j < 8; ++j) {
      int row = kc * 8 + j; bool ok = true;
      if (kind == 3) { int f = 16 * (j >> 2) + 4 * q + (j & 3); ok = f < 28; row = ks * 28 + (ok ? f : 0); }
      v[j] = *(const u32x2*)(src + (size_t)row * ld + n);
      if (!ok) v[j] = u32x2{0, 0};
    }
    unsigned dst; int r0; bool padz = false;
    if (kind == 0) { int part = n / 448, nn = n % 448, hd = nn / 28, f = nn % 28, nt = 2 * part + (f >> 4);
      r0 = f & 15; dst = O_WQ + ((hd * 14 + ks) * 6 + nt) * 1024; csoff = O_CSQ + (hd * 96 + nt * 16) * 4; padz = (f == 24); }
    else if (kind <= 2) { int c = n / 112, nt = (n % 112) >> 3; r0 = ((n & 4) ? 8 : 0) + (kind == 2 ? 2 : 0); rstep2 = 4;
      dst = O_W13 + ((c * 14 + ks) * 14 + nt) * 1024; csoff = O_CS13 + (c * 224 + nt * 16) * 4; }
    else { int cg = n / 112, nt = (n % 112) >> 4; r0 = n & 15;
      dst = (kind == 3) ? O_WO + ((cg * 16 + ks) * 7 + nt) * 1024 : O_W2 + ((cg * 56 + ks) * 7 + nt) * 1024; }
    dst += q * 256; csoff += r0 * 4;
#pragma unroll
    for (int i = 0; i < 4; ++i) {
      u32x4 g;
#pragma unroll
      for (int j = 0; j < 4; ++j) {
        unsigned a = v[2 * j][i >> 1], c = v[2 * j + 1][i >> 1];
        g[j] = (i & 1) ? __builtin_amdgcn_perm(c, a, 0x07060302u) : __builtin_amdgcn_perm(c, a, 0x05040100u);
        cs[i] += blo(g[j]) + bhi(g[j]);
      }
      *(u32x4*)(ws + dst + (r0 + (i & 1) + (i >> 1) * rstep2) * 16) = g;
      if (padz) *(u32x4*)(ws + dst + (12 + i) * 16) = u32x4{0, 0, 0, 0};
    }
    if (padz && ch == 0) *(f32x4*)(ws + csoff + 16) = f32x4{0.f, 0.f, 0.f, 0.f};
  }
  if (kind <= 2) {
    if (act) { for (int i = 0; i < 4; ++i) red[ch][cq * 4 + i] = cs[i]; }
    __syncthreads();
    if (ch < 8) {
      float s = 0.f, y;
#pragma unroll
      for (int k = 0; k < 28; ++k) s += red[(ch >> 2) * 28 + k][cq * 4 + (ch & 3)];
      s = x32(s, y); s += y;
      if (ch < 4) *(float*)(ws + csoff + ((ch & 1) + (ch >> 1) * rstep2) * 4) = s;
    }
  }
}

struct Ctx { rsrc_t ws; gu32* cnt; char* sm; int w, l, g, c; unsigned v16;
  DEV void fresh() { asm volatile("" : "+v"(l)); v16 = l * 16; } };
#define SB() __builtin_amdgcn_sched_barrier(0)
#define TS(i)
#define TX(i)

DEV void publish(const Ctx& C, int idx) {
  wait_vm<0>(); bar();
  if (threadIdx.x == 0) __hip_atomic_fetch_add(C.cnt + (idx * 16 + C.g) * 64, 1u, __ATOMIC_RELAXED, __HIP_MEMORY_SCOPE_AGENT);
}
DEV unsigned peek(gu32* p) { return __hip_atomic_load(p, __ATOMIC_RELAXED, __HIP_MEMORY_SCOPE_AGENT); }
DEV bool full(unsigned v) { return (unsigned)__builtin_amdgcn_readfirstlane(v) >= 16u; }
DEV void poll(gu32* p) {
  if (POLLP) {
    unsigned a = peek(p), b;
#pragma unroll 1
    for (unsigned s = 0; s < 200000u; ++s) {
      __builtin_amdgcn_s_sleep(4); b = peek(p); if (full(a)) break;
      __builtin_amdgcn_s_sleep(4); a = peek(p); if (full(b)) break;
    }
  } else {
#pragma unroll 1
    for (unsigned s = 0; s < 400000u; ++s) { if (full(peek(p))) break; __builtin_amdgcn_s_sleep(1); }
  }
}
DEV void await(const Ctx& C, int idx, bool halo) {
  if (C.w == 0) { poll(C.cnt + (idx * 16 + C.g) * 64); if (halo && (C.g & 7)) poll(C.cnt + (idx * 16 + C.g - 1) * 64); }
  bar();
}
template <bool GLU> DEV void stats_to_lds(const Ctx& C, u32x4 a, u32x4 b, int slot) {
  float m0 = __uint_as_float(a[0]), m1 = __uint_as_float(a[2]), m2 = __uint_as_float(b[0]), m3 = __uint_as_float(b[2]);
  float mean = 0.25f * ((m0 + m1) + (m2 + m3));
  m0 -= mean; m1 -= mean; m2 -= mean; m3 -= mean;
  float M2 = (__uint_as_float(a[1]) + __uint_as_float(a[3])) + (__uint_as_float(b[1]) + __uint_as_float(b[3])) + 112.f * ((m0 * m0 + m1 * m1) + (m2 * m2 + m3 * m3));
  const float ve = M2 * (1.f / 448.f) + 1e-5f, rstd = __builtin_amdgcn_rsqf(ve);
  if constexpr (GLU) *(f32x4*)(C.sm + L_ST + slot * 16) = f32x4{-mean, rstd * -1.4426950408889634f, ve, 0.f};
  else *(f32x2*)(C.sm + L_ST + slot * 8) = f32x2{mean, rstd};
}
template <int N> DEV void wq_dma(const Ctx& C, int part) {
  const unsigned gb = opq(O_WQ + (C.c * 84 + part * N) * 1024), lb = opq(part * N * 1024);
  sfor<0, N>([&](auto I) { dma16<false, I.v>(C.ws, C.v16, gb, lb); });
}
struct Win { f32x4 bp, bo; u32x2 mp, mo; };
DEV Win window(int l) {
  const int d = (l & 15) - 4 * (l >> 4); Win W; unsigned p[4], o[4];
#pragma unroll
  for (int i = 0; i < 4; ++i) { const bool vp = d - i + 16 <= 7, vo = (unsigned)(d - i) <= 7u;
    W.bp[i] = vp ? 0.f : -3e38f; W.bo[i] = vo ? 0.f : -3e38f; p[i] = vp ? 0xffffu << (16 * (i & 1)) : 0u; o[i] = vo ? 0xffffu << (16 * (i & 1)) : 0u; }
  W.mp = u32x2{p[0] | p[1], p[2] | p[3]}; W.mo = u32x2{o[0] | o[1], o[2] | o[3]};
  return W;
}
DEV float vmax(float a, float b) { float r; asm("v_max_f32 %0, %1, %2" : "=v"(r) : "v"(a), "v"(b)); return r; }
template <class PF> DEV void attend(const Ctx& C, const Win& W, f32x4 bp, u32x4 Q, u32x4 Kp, u32x4 Vp, u32x4 Ko, u32x4 Vo, unsigned dst, PF&& pf) {
  const f32x4 z = {0.f, 0.f, 0.f, 0.f};
  f32x4 s0 = mfma(Kp, Q, bp), s1 = mfma(Ko, Q, W.bo);
  asm volatile("s_nop 7" : "+v"(s0), "+v"(s1));
  f32x4 s;
#pragma unroll
  for (int i = 0; i < 4; ++i) s[i] = vmax(s0[i], s1[i]);
  float m = vmax(vmax(s[0], s[1]), vmax(s[2], s[3])), y;
  pf(IC<0>{});
  m = x32(m, y); m = vmax(m, y); m = x16(m, y); m = vmax(m, y);
  const float sc = 0.18898223650461362f * 1.4426950408889634f;
#pragma unroll
  for (int i = 0; i < 4; ++i) s[i] = __builtin_amdgcn_exp2f((s[i] - m) * sc);
  pf(IC<1>{});
  const float r = __builtin_amdgcn_rcpf(rows_sum((s[0] + s[1]) + (s[2] + s[3])));
  const unsigned p01 = pk(s[0] * r, s[1] * r), p23 = pk(s[2] * r, s[3] * r);
  const u32x4 P = {p01 & W.mp[0], p23 & W.mp[1], p01 & W.mo[0], p23 & W.mo[1]};
  f32x4 o0 = mfma(u32x4{Vp[0], Vp[1], Vo[0], Vo[1]}, P, z), o1 = mfma(u32x4{Vp[2], Vp[3], Vo[2], Vo[3]}, P, z);
  st16<16>(u32x4{pk(o0[0], o0[1]), pk(o0[2], o0[3]), pk(o1[0], o1[1]), pk(o1[2], o1[3])}, C.ws, C.v16, dst);
}
DEV u32x2 normT(f32x4 a, f32x4 cs, f32x2 st) {
  return u32x2{pk(st[1] * (a[0] - st[0] * cs[0]), st[1] * (a[1] - st[0] * cs[1])), pk(st[1] * (a[2] - st[0] * cs[2]), st[1] * (a[3] - st[0] * cs[3]))};
}
DEV u32x2 normF(f32x4 a, float cs, f32x4 s01, f32x4 s23) {
  return u32x2{pk(s01[1] * (a[0] - s01[0] * cs), s01[3] * (a[1] - s01[2] * cs)), pk(s23[1] * (a[2] - s23[0] * cs), s23[3] * (a[3] - s23[2] * cs))};
}
template <bool FINAL, class PF> DEV void ksplit_epilogue(const Ctx& C, f32x4 (&acc)[2][7], u32x4 (&res)[2], unsigned xdst, unsigned stdst, rsrc_t out, PF&& pf) {
  const int q = C.l >> 4, tok = C.l & 15;
  sfor<0, 14>([&](auto I) { constexpr int m = I.v / 7, nt = I.v % 7;
    *(f32x4*)(C.sm + L_RED + (C.w * 14 + I.v) * 1024 + q * 256 + ((tok + 8 * (nt & 1)) & 15) * 16) = acc[m][nt];
    if constexpr (I.v % 3 == 1) pf(IC<I.v / 3>{}); });
  bar();
  const int g8 = C.l >> 3, m = C.w >> 1, tk = 8 * (C.w & 1) + (C.l & 7), cg = ROLE_CG(C.c), mt = C.g * 8 + ROLE_MP(C.c) * 2 + m;
  float val[2][8]; float s = 0.f;
  sfor<0, 2>([&](auto H) { constexpr int h = H.v;
    const int gr = min(g8 + 8 * h, 13), nt = gr >> 1;
    const char* p = C.sm + L_RED + (m * 7 + nt) * 1024 + (gr & 1) * 512 + ((tk + 8 * (nt & 1)) & 15) * 16;
    f32x4 a = *(const f32x4*)p, b = *(const f32x4*)(p + 256);
#pragma unroll
    for (int k = 1; k < 4; ++k) { a += *(const f32x4*)(p + k * 14336); b += *(const f32x4*)(p + k * 14336 + 256); }
    pf(IC<5 + 2 * h>{});
    u32x4 r = res[h];
    r = u32x4{pk(a[0] + blo(r[0]), a[1] + bhi(r[0])), pk(a[2] + blo(r[1]), a[3] + bhi(r[1])), pk(b[0] + blo(r[2]), b[1] + bhi(r[2])), pk(b[2] + blo(r[3]), b[3] + bhi(r[3]))};
    res[h] = r;
    pf(IC<6 + 2 * h>{});
    const bool ok = h == 0 || g8 < 6;
    const int G = cg * 14 + gr;
    if constexpr (FINAL) { const int u = mt * 16 + tk; if (ok) st16<0>(r, out, (2 * (u & 1023) + (u >> 10)) * 896 + G * 16, 0); }
    else {
      if (ok) st16<16>(r, C.ws, (mt * 14 + (G >> 2)) * 1024 + (G & 3) * 256 + tk * 16, xdst);
#pragma unroll
      for (int j = 0; j < 4; ++j) { val[h][2 * j] = blo(r[j]); val[h][2 * j + 1] = bhi(r[j]); s += ok ? val[h][2 * j] + val[h][2 * j + 1] : 0.f; }
    }
  });
  pf(IC<9>{});
  if constexpr (!FINAL) {
    const float mean = sum8(s) * (1.f / 112.f); float m2 = 0.f, m2b = 0.f;
#pragma unroll
    for (int j = 0; j < 8; ++j) { float a = val[0][j] - mean, b = val[1][j] - mean; m2 += a * a; m2b += b * b; }
    m2 = sum8(g8 < 6 ? m2 + m2b : m2);
    asm volatile("" : "+v"(res[0]), "+v"(res[1]));
    if (g8 == 0) __builtin_amdgcn_raw_buffer_store_b64(u32x2{__float_as_uint(mean), __float_as_uint(m2)}, C.ws, (mt * 16 + tk) * 32 + cg * 8, stdst, 16);
  }
}

__global__ __launch_bounds__(256, 1) void main_kernel(char* wsp, uint16_t* outp) {
  __shared__ __attribute__((aligned(1024))) char sm[L_END + 256];
  Ctx C;
  C.ws = __builtin_amdgcn_make_buffer_rsrc(wsp, 0, WS_END, 0x00020000);
  const rsrc_t out = __builtin_amdgcn_make_buffer_rsrc(outp, 0, 2048 * 448 * 2, 0x00020000);
  C.cnt = (gu32*)wsp; C.sm = sm;
  C.w = __builtin_amdgcn_readfirstlane(threadIdx.x >> 6); C.l = threadIdx.x & 63; C.v16 = C.l * 16;
  { const int b = blockIdx.x, x = b & 7, s = b >> 3;
    if (XMAP == 0) { C.g = 2 * x + (s >> 4); C.c = s & 15; } else if (XMAP == 2) { C.g = 8 * (x >> 2) + (s >> 2); C.c = (x & 3) * 4 + (s & 3); } else { C.g = 4 * (x >> 1) + (s >> 3); C.c = (x & 1) * 8 + (s & 7); } }
  const int w = C.w, g = C.g, c = C.c, cg = ROLE_CG(c);
  const f32x4 z4 = {0.f, 0.f, 0.f, 0.f};
  const unsigned mp0 = g * 8 + ROLE_MP(c) * 2;
  {
    unsigned long long pc; int len;
    asm volatile("s_getpc_b64 %0\n.Lcp_beg:\n\ts_mov_b32 %1, .Lcp_end-.Lcp_beg" : "=s"(pc), "=s"(len));
    const rsrc_t cr = __builtin_amdgcn_make_buffer_rsrc((void*)pc, 0, max(len, 0) & ~3, 0x00020000);
    const unsigned vo = C.l < 3 ? (((blockIdx.x >> 3) * 4 + w) * 3 + C.l) * 128 : 0x7fffff00u;
    asm volatile("s_mov_b32 m0, %0\n\ts_nop 0\n\tbuffer_load_dword %1, %2, 0 offen lds" :: "s"(L_END), "v"(vo), "s"(cr) : "memory");
  }
  wq_dma<21>(C, w);
  u32x4 res[2];
#pragma unroll
  for (int h = 0; h < 2; ++h) { const int l = C.l, G = cg * 14 + min((l >> 3) + 8 * h, 13);
    res[h] = ld16<0>(C.ws, ((mp0 + (w >> 1)) * 14 + (G >> 2)) * 1024 + (G & 3) * 256 + (8 * (w & 1) + (l & 7)) * 16, O_X); }

#pragma unroll 1
  for (int L = 0; L < 2; ++L) {
    u32x4 wo[28], w13[56], w2[98];
    {
      C.fresh(); const int q = C.l >> 4, tok = C.l & 15, t = w * 64 + C.l;
      const unsigned Xin = O_X + 2 * L * X_SZ, Sin = O_ST + 2 * L * ST_SZ;
      const f32x4 csq0 = __builtin_bit_cast(f32x4, ld16<0>(C.ws, q * 16, O_CSQ + c * 384)), csq1 = __builtin_bit_cast(f32x4, ld16<0>(C.ws, q * 16, O_CSQ + c * 384 + 64));
      const f32x4 csk0 = __builtin_bit_cast(f32x4, ld16<0>(C.ws, q * 16, O_CSQ + c * 384 + 128)), csk1 = __builtin_bit_cast(f32x4, ld16<0>(C.ws, q * 16, O_CSQ + c * 384 + 192));
      const float csv0 = __uint_as_float(__builtin_amdgcn_raw_buffer_load_b32(C.ws, tok * 4, O_CSQ + c * 384 + 256, 0));
      const float csv1 = __uint_as_float(__builtin_amdgcn_raw_buffer_load_b32(C.ws, tok * 4, O_CSQ + c * 384 + 320, 0));
      TS(0); TX(0);
      if (L) await(C, 3, true);
      TS(1); TX(1);
      const unsigned mh = (g & 7) ? 8 * g - 1 : 8 * g, ma = 8 * g + 2 * w;
      const int sl = min(t, 143); const unsigned su = g * 128 - 16 + sl + (sl < ((g & 7) ? 0 : 16) ? 16 : 0);
      u32x4 sa = ld16<16>(C.ws, su * 32, Sin), sb = ld16<16>(C.ws, su * 32 + 16, Sin);
      u32x4 xh[4], xa[14], xb[14];
      const unsigned hb = opq(Xin + mh * 14336 + w * 1024), h3 = opq(Xin + mh * 14336 + (12 + (w & 1)) * 1024), ab_ = opq(Xin + ma * 14336), wob = opq(O_WO + (cg * 16 + w * 4) * 7168);
      sfor<0, NX1>([&](auto I) { xa[I.v] = ldf<16, I.v>(C.ws, C.v16, ab_); xb[I.v] = ldf<16, 14 + I.v>(C.ws, C.v16, ab_); });
      wait_vm<2 * NX1>(); stats_to_lds<false>(C, sa, sb, sl); bar();
      TX(2);
      const char* wl = sm + C.v16;
      f32x4 aa[6], ab[6], ah[4];
      sfor<0, 6>([&](auto N) { aa[N.v] = z4; ab[N.v] = z4; ah[N.v & 3] = z4; });
      constexpr int D1 = 5; u32x4 W[100];
      const char* wh = wl + w * 6144; const char* wh3 = wl + (12 + (w & 1)) * 6144;
      auto rdw = [&](auto I) { constexpr int j = I.v - 84;
        if constexpr (j < 0) W[I.v] = *(const u32x4*)(wl + I.v * 1024);
        else if constexpr (j < 12) W[I.v] = *(const u32x4*)(wh + (j / 4 * 24 + 2 + j % 4) * 1024);
        else if constexpr (j < 16) W[I.v] = *(const u32x4*)(wh3 + (j - 10) * 1024); };
      sfor<0, D1>(rdw);
      SB();
      sfor<0, 84>([&](auto I) { constexpr int k = I.v / 6, n = I.v % 6;
        rdw(IC<I.v + D1>{});
        if constexpr (n == 0 && k + NX1 < 14) xa[k + NX1] = ldf<16, k + NX1>(C.ws, C.v16, ab_);
        if constexpr (n == 2 && k + NX1 < 14) xb[k + NX1] = ldf<16, 14 + k + NX1>(C.ws, C.v16, ab_);
        if constexpr (n == 4 && k >= 4 && k < 8) xh[k - 4] = ld16<16>(C.ws, C.v16, k < 7 ? hb + (k - 4) * 4096 : h3);
        if constexpr ((n == 1 || n == 5) && k >= 14 - NWO) wo[2 * (k - 14 + NWO) + n / 4] = ldf<0, 2 * (k - 14 + NWO) + n / 4>(C.ws, C.v16, wob);
        if constexpr (n < 4) { aa[n] = mfma(W[I.v], xa[k], aa[n]); ab[n] = mfma(W[I.v], xb[k], ab[n]); }
        else { aa[n] = mfma(xa[k], W[I.v], aa[n]); ab[n] = mfma(xb[k], W[I.v], ab[n]); }
        if constexpr (I.v == 5) { TX(3); } if constexpr (I.v == 41) { TX(4); }
        SB(); });
      if (w >= 2) xh[3] = u32x4{0, 0, 0, 0};
      sfor<0, 16>([&](auto J) { constexpr int n = J.v % 4;
        rdw(IC<84 + J.v + D1>{});
        if constexpr (n < 2) ah[n] = mfma(W[84 + J.v], xh[J.v / 4], ah[n]); else ah[n] = mfma(xh[J.v / 4], W[84 + J.v], ah[n]);
        SB(); });
      sfor<0, 4>([&](auto N) { *(f32x4*)(sm + L_RED + (w * 4 + N.v) * 1024 + C.v16) = ah[N.v]; });
      TS(2); TX(5); bar();
      const char* sp = sm + L_ST;
      auto pwo = [&](auto I) { constexpr int j = 2 * (I.v + NWO); if constexpr (j < 28) { SB(); wo[j] = ldf<0, j>(C.ws, C.v16, wob); wo[j + 1] = ldf<0, j + 1>(C.ws, C.v16, wob); SB(); } };
      pwo(IC<0>{});
      { u32x2 hv; const char* hp = sm + L_RED + w * 1024 + C.v16;
        const f32x4 hs = (*(const f32x4*)hp + *(const f32x4*)(hp + 4096)) + (*(const f32x4*)(hp + 8192) + *(const f32x4*)(hp + 12288));
        if (w < 2) hv = normT(hs, w ? csk1 : csk0, *(const f32x2*)(sp + tok * 8));
        else hv = normF(hs, w == 2 ? csv0 : csv1, *(const f32x4*)(sp + q * 32), *(const f32x4*)(sp + q * 32 + 16));
        *(u32x2*)(sm + L_EX + (w >> 1) * 1024 + C.v16 + (w & 1) * 8) = hv; }
      auto cat = [](u32x2 a, u32x2 b) { return u32x4{a[0], a[1], b[0], b[1]}; };
      const char* spb = sp + (32 + 32 * w) * 8; const char* spa = spb - 128;
      const f32x2 tb = *(const f32x2*)(spb + tok * 8), ta = *(const f32x2*)(spa + tok * 8);
      const f32x4 b01 = *(const f32x4*)(spb + q * 32), b23 = *(const f32x4*)(spb + q * 32 + 16), a01 = *(const f32x4*)(spa + q * 32), a23 = *(const f32x4*)(spa + q * 32 + 16);
      auto nT = [&](f32x4 a0, f32x4 a1, f32x4 c0, f32x4 c1, f32x2 st, auto I) { pwo(I); u32x2 x = normT(a0, c0, st); pwo(IC<I.v + 1>{}); return cat(x, normT(a1, c1, st)); };
      auto nF = [&](f32x4 a0, f32x4 a1, f32x4 s01, f32x4 s23, auto I) { pwo(I); u32x2 x = normF(a0, csv0, s01, s23); pwo(IC<I.v + 1>{}); return cat(x, normF(a1, csv1, s01, s23)); };
      const u32x4 Kb = nT(ab[2], ab[3], csk0, csk1, tb, IC<1>{}), Vb = nF(ab[4], ab[5], b01, b23, IC<3>{});
      *(u32x4*)(sm + L_EX + (w + 1) * 2048 + C.v16) = Kb; *(u32x4*)(sm + L_EX + (w + 1) * 2048 + 1024 + C.v16) = Vb;
      const u32x4 Ka = nT(aa[2], aa[3], csk0, csk1, ta, IC<5>{}), Va = nF(aa[4], aa[5], a01, a23, IC<7>{});
      const u32x4 Qb = nT(ab[0], ab[1], csq0, csq1, tb, IC<9>{});
      const unsigned od = O_O + L * O_SZ + (ma * 16 + c) * 1024;
      const Win win = window(C.l);
      attend(C, win, win.bp, Qb, Ka, Va, Kb, Vb, od + 16384, [&](auto I) { pwo(IC<11 + I.v>{}); });
      pwo(IC<13>{});
      const u32x4 Qa = cat(normT(aa[0], csq0, ta), normT(aa[1], csq1, ta));
      bar();
      const u32x4 Kp = *(const u32x4*)(sm + L_EX + w * 2048 + C.v16), Vp = *(const u32x4*)(sm + L_EX + w * 2048 + 1024 + C.v16);
      attend(C, win, (w > 0 || (g & 7)) ? win.bp : f32x4{-3e38f, -3e38f, -3e38f, -3e38f}, Qa, Kp, Vp, Ka, Va, od, [](auto) {});
      TS(3); publish(C, L * 4 + 0); TS(4);
    }
    const unsigned b13 = opq(O_W13 + (c * 196 + 3 * w) * 1024), b13x = opq(O_W13 + (c * 196 + 12 + (w >> 1)) * 1024), b2 = opq(O_W2 + (cg * 56 + w * 14) * 7168);
    auto ld13 = [&](auto I) { constexpr int ks = I.v / 4, j = I.v % 4;
      w13[I.v] = ld16<0>(C.ws, C.v16 + (j < 3 ? j * 1024 : 0), (j < 3 ? b13 : b13x) + ks * 14336); };
    auto ld2 = [&](auto I) { if constexpr (I.v < 63) w2[35 + I.v] = ldf<0, 35 + I.v>(C.ws, C.v16, b2); };
    const unsigned la = opq(w * 21504), lb = opq(L_RED + w * 14336);
    auto dm2 = [&](auto I) { if constexpr (I.v < 21) dma16<false, I.v>(C.ws, C.v16, b2, la); else if constexpr (I.v < 35) dma16<false, I.v - 21>(C.ws, C.v16, b2 + 21504, lb); };
    {
      C.fresh();
      sfor<0, N13A>(ld13);
      await(C, L * 4 + 0, false); TS(5);
      u32x4 of[2][4];
      const unsigned ob = opq(O_O + L * O_SZ + (mp0 * 16 + w * 4) * 1024);
      sfor<0, 8>([&](auto I) { of[I.v & 1][I.v >> 1] = ldf<16, (I.v & 1) * 16 + (I.v >> 1)>(C.ws, C.v16, ob); });
      sfor<N13A, N13A + N13C>(ld13);
      SB();
      f32x4 acc[2][7]; constexpr int N13D = N13A + N13C + 14;
      sfor<0, 14>([&](auto I) { acc[I.v / 7][I.v % 7] = z4; });
      sfor<0, 4>([&](auto K) { sfor<0, 7>([&](auto N) { acc[0][N.v] = mfma(wo[K.v * 7 + N.v], of[0][K.v], acc[0][N.v]); acc[1][N.v] = mfma(wo[K.v * 7 + N.v], of[1][K.v], acc[1][N.v]);
        pin_a(acc[1][N.v]); if constexpr ((K.v * 7 + N.v) % 2 == 0) ld13(IC<N13A + N13C + (K.v * 7 + N.v) / 2>{}); SB(); }); });
      C.fresh();
      ksplit_epilogue<false>(C, acc, res, O_X + (2 * L + 1) * X_SZ, O_ST + (2 * L + 1) * ST_SZ, out,
                             [&](auto I) { SB(); sfor<N13D + N13B * I.v, N13D + N13B * (I.v + 1)>(ld13); SB(); });
      TS(6); publish(C, L * 4 + 1); TS(7);
    }
    {
      C.fresh();
      sfor<N13A + N13C + 14 + 10 * N13B, 56>(ld13);
      await(C, L * 4 + 1, false); TS(8); TX(6);
      const unsigned Xin = O_X + (2 * L + 1) * X_SZ, Sin = O_ST + (2 * L + 1) * ST_SZ;
      const int sl = (w & 1) * 64 + C.l; const unsigned su = g * 128 + sl;
      u32x4 sa = ld16<16>(C.ws, su * 32, Sin), sb = ld16<16>(C.ws, su * 32 + 16, Sin);
      const unsigned xg = opq(Xin + (g * 8 + w) * 14336), xs = opq(w * 14336);
      auto dmx = [&](auto I) { constexpr int h = I.v / 14, ks = I.v % 14; if constexpr (I.v < 28) dma16<true, ks>(C.ws, C.v16, xg + h * 57344, xs + h * 57344); };
      sfor<0, 6>(dmx);
      f32x4 acc[8][3], ax[4], cs[4], st[8];
      sfor<0, 8>([&](auto M) { acc[M.v][0] = z4; acc[M.v][1] = z4; acc[M.v][2] = z4; ax[M.v & 3] = z4; });
      const int q = C.l >> 4, tok = C.l & 15, mx = 2 * (w & 1);
      const char* x0 = sm + C.v16 + mx * 14336; const char* x1 = sm + C.v16 + (mx ^ 2) * 14336;
      constexpr int D3 = 4; u32x4 X[112];
      unsigned xo0 = C.v16 + mx * 14336 + 57344, xo1 = C.v16 + (mx ^ 2) * 14336 + 57344;
      asm volatile("" : "+v"(xo0), "+v"(xo1));
      auto rdx = [&](auto I) { if constexpr (I.v < 56) X[I.v] = *(const u32x4*)(((I.v & 2) ? x1 : x0) + (I.v & 1) * 14336 + I.v / 4 * 1024);
        else if constexpr (I.v < 112) X[I.v] = *(const u32x4*)(sm + ((I.v & 2) ? xo1 : xo0) + (I.v & 1) * 14336 + (I.v - 56) / 4 * 1024); };
      auto rst = [&](auto M) { st[M.v] = *(const f32x4*)(sm + L_ST + ((M.v ^ mx) * 16 + tok) * 16); };
      const unsigned Hd = opq(O_H + L * H_SZ + g * 8 * 57344), hv0 = (q ^ mx) * 57344 + tok * 16;
      float r0, r1, r2, r3, e0, e1; unsigned hv[4]; u32x2 ac, bd, y01, y23;
      auto item = [&](auto S, const f32x4& a, const f32x4& cn, const f32x4& sv, unsigned& h) { constexpr int t = S.v;
        if constexpr (t == 0) { r0 = a_rd(a[0]); r1 = a_rd(a[1]); }
        if constexpr (t == 1) r0 = a_fma(sv[0], cn[0], r0);
        if constexpr (t == 2) { r1 = a_fma(sv[0], cn[1], r1); e0 = a_mul(r0, sv[1]); }
        if constexpr (t == 3) e1 = a_mul(r1, sv[1]);
        if constexpr (t == 4) { e0 = a_exp(e0); r2 = a_rd(a[2]); }
        if constexpr (t == 5) { e1 = a_exp(e1); r3 = a_rd(a[3]); }
        if constexpr (t == 6) { r2 = a_fma(sv[0], cn[2], r2); r3 = a_fma(sv[0], cn[3], r3); }
        if constexpr (t == 7) e0 = a_fma(e0, sv[2], sv[2]);
        if constexpr (t == 8) { e1 = a_fma(e1, sv[2], sv[2]); e0 = a_rcp(e0); }
        if constexpr (t == 9) { e1 = a_rcp(e1); r0 = a_mul(r0, r2); }
        if constexpr (t == 10) r1 = a_mul(r1, r3);
        if constexpr (t == 11) { r0 = a_mul(r0, e0); r1 = a_mul(r1, e1); }
        if constexpr (t == 12) { h = pk(r0, r1); asm volatile("" :: "v"(h)); } };
      auto glu = [&](const f32x4& a, const f32x4& cn, const f32x4& sv) {
        const f32x2 tg = f32x2{a[0], a[1]} + sv[0] * f32x2{cn[0], cn[1]}, tu = f32x2{a[2], a[3]} + sv[0] * f32x2{cn[2], cn[3]}, ar = tg * sv[1];
        const f32x2 dn = f32x2{__builtin_amdgcn_exp2f(ar[0]), __builtin_amdgcn_exp2f(ar[1])} * sv[2] + sv[2];
        const f32x2 hh = tg * tu * f32x2{__builtin_amdgcn_rcpf(dn[0]), __builtin_amdgcn_rcpf(dn[1])};
        return pk(hh[0], hh[1]); };
      auto xpose = [&](auto S, int nt, unsigned voff, unsigned soff) { constexpr int t = S.v;
        if constexpr (t == 1) ac = __builtin_amdgcn_permlane32_swap(hv[0], hv[2], false, false);
        if constexpr (t == 3) bd = __builtin_amdgcn_permlane32_swap(hv[1], hv[3], false, false);
        if constexpr (t == 7) y01 = __builtin_amdgcn_permlane16_swap(ac[0], bd[0], false, false);
        if constexpr (t == 10) y23 = __builtin_amdgcn_permlane16_swap(ac[1], bd[1], false, false);
        if constexpr (t == 13) { const unsigned G = 14 * c + nt; st16<16>(u32x4{y01[0], y01[1], y23[0], y23[1]}, C.ws, voff, soff + (G >> 2) * 1024 + (G & 3) * 256); } };
      wait_vm<4>(); stats_to_lds<true>(C, sa, sb, sl); bar(); TX(7);
      sfor<0, D3>(rdx);
      SB();
      sfor<0, 112>([&](auto I) { constexpr int h = I.v / 56, k = I.v / 4 % 14, m = I.v % 4, t0 = m < 2 ? 4 * m : 2 + 3 * m;
        if constexpr (I.v == 4) { wait_vm<2>(); bar(); TX(8); }
        if constexpr (I.v == 20) { wait_vm<6>(); bar(); }
        if constexpr (I.v == 36) { wait_vm<10>(); bar(); TX(9); }
        if constexpr (I.v == 52) { wait_vm<10 + 2 * N2A>(); bar(); }
        if constexpr (I.v == 84) { wait_vm<4 + 3 * N2A + 6 * PF3>(); bar(); TX(10); }
        rdx(IC<I.v + D3>{});
        sfor<0, (m < 2 ? 4 : 3)>([&](auto J) { constexpr int t = t0 + J.v;
          f32x4& d = J.v < 3 ? acc[4 * h + m][J.v % 3] : ax[2 * h + m % 2];
          d = mfma(w13[k * 4 + J.v], X[I.v], d); pin_a(d);
          if constexpr (h == 0) {
            if constexpr (t == 1 || t == 8) dmx(IC<6 + 2 * k + t / 8>{});
            if constexpr (k >= 11 && t % 4 == 2 && t / 4 < N2A) ld2(IC<(k - 11) * N2A + t / 4>{});
            if constexpr (k == 11 && t % 4 == 0) cs[t / 4] = __builtin_bit_cast(f32x4, ld16<0>(C.ws, q * 16, O_CS13 + (c * 224 + (t < 12 ? 3 * w + t / 4 : 12 + (w >> 1)) * 16) * 4));
            if constexpr (k == 13 && t % 4 == 0) rst(IC<t / 4>{});
          } else {
            if constexpr (k > 0 && t % 3 == 1 && t / 3 < PF3) ld2(IC<3 * N2A + (k - 1) * PF3 + t / 3>{});
            if constexpr (k >= 1 && k <= 12) item(IC<t>{}, acc[(k - 1) % 4][(k - 1) / 4], cs[(k - 1) / 4], st[(k - 1) % 4], hv[(k - 1) % 4]);
            if constexpr (k == 5 || k == 9 || k == 13) xpose(IC<t>{}, 3 * w + (k - 5) / 4, hv0, Hd);
            if constexpr (k == 13 && t % 4 == 0) rst(IC<4 + t / 4>{});
          }
          SB(); }); });
      bar();
      TS(9); TX(11);
      sfor<0, 4>([&](auto U) { constexpr int j = U.v;
        sfor<ND2 * j, ND2 * (j + 1)>(dm2); SB();
        sfor<0, 4>([&](auto I) { constexpr int i = I.v;
          if constexpr (j < 3) hv[i] = glu(acc[4 + i][j], cs[j], st[4 + i]); else hv[i] = glu(ax[i], cs[3], st[(i & 1) + 4 * (i >> 1)]); });
        sfor<0, 14>([&](auto S) { if constexpr (j < 3) xpose(S, 3 * w + j, hv0, Hd + 4 * 57344);
          else xpose(S, 12 + (w >> 1), ((q & 1) + mx + 4 * (q >> 1)) * 57344 + tok * 16, Hd); });
        SB(); });
      TS(10); publish(C, L * 4 + 2); TS(11);
    }
    {
      C.fresh();
      constexpr int D0 = 4 * ND2 + ND3 + ND4;
      sfor<3 * N2A + 13 * PF3, 63>(ld2); sfor<4 * ND2, D0 - ND4>(dm2);
      await(C, L * 4 + 2, false); TS(12); TX(12);
      const unsigned Hs = opq(O_H + L * H_SZ + (mp0 * 56 + w * 14) * 1024);
      u32x4 hf[2][14];
      auto ldh = [&](auto M, auto S) { constexpr int k = (S.v + 5) % 14; if constexpr (S.v < 14) hf[M.v][k] = ldf<16, 56 * M.v + k>(C.ws, C.v16, Hs); };
      sfor<0, NH0>([&](auto S) { ldh(IC<0>{}, S); ldh(IC<1>{}, S); });
      auto dm2t = [&](auto I) { constexpr int f = I.v < 21 ? I.v : I.v - 21;
        if constexpr (I.v < 35) __builtin_amdgcn_raw_ptr_buffer_load_lds(C.ws, (__attribute__((address_space(3))) void*)(uintptr_t)((I.v < 21 ? la : lb) + (f >> 2) * 4096), 16, C.v16,
                                                                        b2 + (I.v < 21 ? 0 : 21504) + (f >> 2) * 4096, (f & 3) * 1024, 0); };
      sfor<D0 - ND4, D0>(dm2t);
      const char* pa = sm + w * 21504 + C.v16; const char* pb = sm + L_RED + w * 14336 - 21504 + C.v16;
      constexpr int D4 = 4, SD = (35 - D0 + 2) / 3, NY = 6 + 2 * ((8 < 14 - NH0 ? 8 : 14 - NH0) - SD);
      static_assert(SD <= 6 && SD <= 14 - NH0, "count of loads younger than the W2 DMAs");
      auto rd2 = [&](auto I) { if constexpr (I.v >= 0 && I.v < 35) w2[I.v] = *(const u32x4*)((I.v < 21 ? pa : pb) + I.v * 1024); };
      SB();
      f32x4 acc[2][7]; u32x4 wq[21];
      const unsigned bq = opq(L ? WS_END : O_WQ + (c * 84 + w * 21) * 1024);
      sfor<0, 14>([&](auto I) { acc[I.v / 7][I.v % 7] = z4; });
      sfor<0, 14>([&](auto S) { constexpr int k = (S.v + 5) % 14;
        if constexpr (S.v == 8) wait_vm<(NY > 0 ? NY : 0)>();
        sfor<0, 7>([&](auto N) {
          acc[0][N.v] = mfma(w2[k * 7 + N.v], hf[0][k], acc[0][N.v]); pin_a(acc[0][N.v]);
          if constexpr (N.v % 2 == 0 && N.v < 6) dm2t(IC<D0 + 3 * S.v + N.v / 2>{});
          rd2(IC<(S.v - 9) * 7 + N.v + D4>{});
          SB();
          acc[1][N.v] = mfma(w2[k * 7 + N.v], hf[1][k], acc[1][N.v]); pin_a(acc[1][N.v]);
          if constexpr (S.v >= 6 && S.v < 13 && N.v % 2 == 0 && N.v < 6) wq[3 * (S.v - 6) + N.v / 2] = ldf<0, 3 * (S.v - 6) + N.v / 2>(C.ws, C.v16, bq);
          if constexpr (N.v == 5) ldh(IC<0>{}, IC<S.v + NH0>{});
          if constexpr (N.v == 6) ldh(IC<1>{}, IC<S.v + NH0>{});
          SB(); });
        if constexpr (S.v == 0) { TX(13); } if constexpr (S.v == 6) { TX(14); }
      });
      TS(13); TX(15); C.fresh();
      if (L == 0) { ksplit_epilogue<false>(C, acc, res, O_X + 2 * X_SZ, O_ST + 2 * ST_SZ, out, [](auto) {}); TS(14); publish(C, 3); TS(15);
        sfor<0, 21>([&](auto J) { *(u32x4*)(sm + w * 21504 + J.v * 1024 + C.v16) = wq[J.v]; }); }
      else { ksplit_epilogue<true>(C, acc, res, 0, 0, out, [](auto) {}); TS(14); TS(15); }
    }
  }
  asm volatile(".Lcp_end:");
}

extern "C" void kernel_launch(void* const* d_in, const int* in_sizes, int n_in, void* d_out, int out_size, void* d_ws, size_t ws_size,
                              hipStream_t stream) {
  prep_kernel<<<288, 512, 0, stream>>>((const uint16_t*)d_in[0], (const uint16_t*)d_in[1], (const uint16_t*)d_in[2], (const uint16_t*)d_in[4],
                                       (const uint16_t*)d_in[5], (const uint16_t*)d_in[6], (char*)d_ws);
  main_kernel<<<256, 256, 0, stream>>>((char*)d_ws, (uint16_t*)d_out);
}
```
